```python
import torch

def _softmax(x, axis=-1):
    return torch.softmax(x, dim=axis)
def _gelu(x):
    return torch.nn.functional.gelu(x, approximate="tanh")
def _silu(x):
    return torch.nn.functional.silu(x)
def _sigmoid(x):
    return torch.sigmoid(x)
def _layernorm(x, axis=-1, eps=1e-5):
    m = x.mean(dim=axis, keepdim=True)
    v = x.var(dim=axis, keepdim=True, unbiased=False)
    return (x - m) / torch.sqrt(v + eps)
def _scatter_add(tgt, idx, src, axis=0):
    return tgt.clone().index_add_(axis, idx, src)
def _segment_sum(src, seg, n):
    out = torch.zeros((n,) + src.shape[1:], dtype=src.dtype)
    return out.index_add_(0, seg, src)
def _pad(x, pw):
    flat = tuple(v for pair in reversed(pw) for v in pair)
    return torch.nn.functional.pad(x, flat)

kernel_name = 'gated_mix__mha_block_cached__dense_activation_relu__decode'

def setup_inputs():

    _w = (lambda a: (lambda t: t.to(torch.bfloat16) if t.is_floating_point() else t)(torch.as_tensor(a)))
    def _A_setup():
        B, Sq, D = 2, 1, 448
        H, Sk, Dh = 16, 4096, 256
        return {
            "x": _w((lambda *s: torch.randn(*s, dtype=torch.bfloat16) * 0.05)(B, Sq, D)),
            "k_cache": _w((lambda *s: torch.randn(*s, dtype=torch.bfloat16) * 6.4)(B, H, Sk, Dh)),
            "v_cache": _w((lambda *s: torch.randn(*s, dtype=torch.bfloat16) * 0.4)(B, H, Sk, Dh)),
            "wq": _w((lambda *s: torch.randn(*s, dtype=torch.bfloat16) * 6.4)(D, H * Dh) * (D ** -0.5)),
            "wo": _w((lambda *s: torch.randn(*s, dtype=torch.bfloat16) * 0.4)(H * Dh, D) * ((H * Dh) ** -0.5)),
        }
    _A_in = _A_setup()

    def _B_setup():
        B, S, D, Dff = 2, 1, 448, 1792
        return {
            "x": _w((lambda *s: torch.randn(*s, dtype=torch.bfloat16) * 0.05)(B, S, D)),
            "w1": _w((lambda *s: torch.randn(*s, dtype=torch.bfloat16) * 0.05)(D, Dff)),
            "w2": _w((lambda *s: torch.randn(*s, dtype=torch.bfloat16) * 0.05)(Dff, D)),
        }
    _B_in = _B_setup()

    return {
        "A_x": _A_in["x"],
        "A_k_cache": _A_in["k_cache"],
        "A_v_cache": _A_in["v_cache"],
        "A_wq": _A_in["wq"],
        "A_wo": _A_in["wo"],
        "B_x": _B_in["x"],
        "B_w1": _B_in["w1"],
        "B_w2": _B_in["w2"],
        "gate_w": _w((lambda *s: torch.randn(*s, dtype=torch.bfloat16) * 0.05)(448) * 16.0),
    }

def reference(A_x, A_k_cache, A_v_cache, A_wq, A_wo, B_x, B_w1, B_w2, gate_w):
    def _A_ref(x, k_cache, v_cache, wq, wo):
        B, Sq, D = x.shape
        H, Dh = 16, 256
        q = (lambda x, *d: x.permute(*d))((x @ wq).reshape(B, Sq, H, Dh), 0, 2, 1, 3)
        scores = torch.einsum("bhqd,bhkd->bhqk", q, k_cache) * (Dh ** -0.5)
        p = _softmax(scores, axis=-1)
        o = torch.einsum("bhqk,bhkd->bhqd", p, v_cache)
        return (lambda x, *d: x.permute(*d))(o, 0, 2, 1, 3).reshape(B, Sq, H * Dh) @ wo

    def _B_ref(x, w1, w2):
        h = torch.einsum("bsd,df->bsf", x, w1)
        zero = torch.zeros_like(h)
        h = torch.maximum(h, zero)
        return torch.einsum("bsf,fd->bsd", h, w2)

    x = A_x
    g = _sigmoid((x * gate_w).sum(-1, keepdims=True))
    a = _A_ref(x, A_k_cache, A_v_cache, A_wq, A_wo)
    b = _B_ref(x, B_w1, B_w2)
    return g * a + (1 - g) * b
```

```cpp
#include <hip/hip_runtime.h>
#include <hip/hip_bf16.h>
#include <stdint.h>

typedef unsigned int u32;
typedef __attribute__((ext_vector_type(4))) unsigned u32x4;
typedef __attribute__((ext_vector_type(2))) __bf16 bf16x2;

constexpr int B = 2, D = 448, H = 16, SK = 4096, DH = 256, DFF = 1792;
constexpr int NSPLIT = 8, KEYS = SK / NSPLIT;

__device__ __forceinline__ float bf2f(unsigned short v) { return __uint_as_float(((u32)v) << 16); }
__device__ __forceinline__ float lo2f(u32 v) { return __uint_as_float(v << 16); }
__device__ __forceinline__ float hi2f(u32 v) { return __uint_as_float(v & 0xffff0000u); }
__device__ __forceinline__ float dot2(u32 a, u32 b, float c) {
  return __builtin_amdgcn_fdot2_f32_bf16(__builtin_bit_cast(bf16x2, a), __builtin_bit_cast(bf16x2, b), c, false);
}

__global__ void k_pre(const unsigned short* x, const unsigned short* wq, const unsigned short* w1,
                      __hip_bfloat16* q, float* hbuf) {
  int col = blockIdx.x * 64 + threadIdx.x;
  float a0 = 0.f, a1 = 0.f;
  if (col < H * DH) {
    for (int k = 0; k < D; ++k) { float w = bf2f(wq[k * (H * DH) + col]); a0 += bf2f(x[k]) * w; a1 += bf2f(x[D + k]) * w; }
    q[col] = __float2bfloat16(a0); q[H * DH + col] = __float2bfloat16(a1);
  } else {
    int c = col - H * DH;
    for (int k = 0; k < D; ++k) { float w = bf2f(w1[k * DFF + c]); a0 += bf2f(x[k]) * w; a1 += bf2f(x[D + k]) * w; }
    hbuf[c] = fmaxf(a0, 0.f); hbuf[DFF + c] = fmaxf(a1, 0.f);
  }
}

__global__ void __launch_bounds__(512) k_attn(const unsigned short* q, const unsigned short* kc, const unsigned short* vc,
                                              float* po, float* pml) {
  __shared__ float s_sc[KEYS];
  __shared__ float s_red[8][DH];
  __shared__ float s_m[8];
  const int bh = blockIdx.x >> 3, s = blockIdx.x & 7;
  const int tid = threadIdx.x, lane = tid & 63, wave = tid >> 6;
  const int half = lane >> 5, c = lane & 31;
  const size_t base = ((size_t)bh * SK + s * KEYS + wave * 64) * DH;
  const u32x4* kp = (const u32x4*)(kc + base) + lane;
  const u32x4* vp = (const u32x4*)(vc + base) + lane;
  u32x4 qv = *(const u32x4*)(q + bh * DH + c * 8);
  u32x4 buf[32];
#pragma unroll
  for (int i = 0; i < 32; ++i) buf[i] = __builtin_nontemporal_load(kp + i * 64);
  float wm = -INFINITY;
#pragma unroll
  for (int i = 0; i < 32; ++i) {
    float d = dot2(buf[i][0], qv[0], 0.f); d = dot2(buf[i][1], qv[1], d); d = dot2(buf[i][2], qv[2], d); d = dot2(buf[i][3], qv[3], d);
    buf[i] = __builtin_nontemporal_load(vp + i * 64);
    for (int o = 16; o >= 1; o >>= 1) d += __shfl_xor(d, o);
    d *= 0.0625f;
    if (c == 0) s_sc[wave * 64 + i * 2 + half] = d;
    wm = fmaxf(wm, d);
  }
  wm = fmaxf(wm, __shfl_xor(wm, 32));
  if (lane == 0) s_m[wave] = wm;
  __syncthreads();
  float m = s_m[0];
  for (int w = 1; w < 8; ++w) m = fmaxf(m, s_m[w]);
  float acc[8] = {0, 0, 0, 0, 0, 0, 0, 0};
  float l = 0.f;
#pragma unroll
  for (int i = 0; i < 32; ++i) {
    float p = __expf(s_sc[wave * 64 + i * 2 + half] - m);
    l += p;
#pragma unroll
    for (int j = 0; j < 4; ++j) { acc[2 * j] += p * lo2f(buf[i][j]); acc[2 * j + 1] += p * hi2f(buf[i][j]); }
  }
#pragma unroll
  for (int j = 0; j < 8; ++j) acc[j] += __shfl_xor(acc[j], 32);
  l += __shfl_xor(l, 32);
  if (half == 0) {
#pragma unroll
    for (int j = 0; j < 8; ++j) s_red[wave][c * 8 + j] = acc[j];
  }
  __syncthreads();
  if (lane == 0) s_m[wave] = l;
  __syncthreads();
  if (tid < DH) {
    float o = 0.f;
    for (int w = 0; w < 8; ++w) o += s_red[w][tid];
    po[(size_t)blockIdx.x * DH + tid] = o;
  }
  if (tid == 0) {
    float L = 0.f;
    for (int w = 0; w < 8; ++w) L += s_m[w];
    pml[blockIdx.x * 2] = m; pml[blockIdx.x * 2 + 1] = L;
  }
}

__global__ void __launch_bounds__(256) k_post(const float* po, const float* pml, const float* hbuf, const unsigned short* x,
                       const unsigned short* wo, const unsigned short* w2, const unsigned short* gw, __hip_bfloat16* out) {
  __shared__ float s_o[H * DH];
  __shared__ float s_r[256];
  __shared__ float s_r2[256];
  const int b = blockIdx.x / 56, cg = blockIdx.x % 56, tid = threadIdx.x;
  for (int h = 0; h < H; ++h) {
    int bh = b * H + h;
    float M = -INFINITY;
    for (int s = 0; s < NSPLIT; ++s) M = fmaxf(M, pml[(bh * NSPLIT + s) * 2]);
    float L = 0.f, o = 0.f;
    for (int s = 0; s < NSPLIT; ++s) { float w = __expf(pml[(bh * NSPLIT + s) * 2] - M); L += w * pml[(bh * NSPLIT + s) * 2 + 1]; o += w * po[(size_t)(bh * NSPLIT + s) * DH + tid]; }
    s_o[h * DH + tid] = o / L;
  }
  __syncthreads();
  const int n = cg * 8 + (tid & 7), r = tid >> 3;
  float a = 0.f, f = 0.f;
  for (int k = r; k < H * DH; k += 32) a += s_o[k] * bf2f(wo[k * D + n]);
  for (int k = r; k < DFF; k += 32) f += hbuf[b * DFF + k] * bf2f(w2[k * D + n]);
  s_r[tid] = a; s_r2[tid] = f;
  __syncthreads();
  if (tid < 8) {
    float A = 0.f, F = 0.f;
    for (int j = 0; j < 32; ++j) { A += s_r[j * 8 + tid]; F += s_r2[j * 8 + tid]; }
    float z = 0.f;
    for (int k = 0; k < D; ++k) z += bf2f(x[b * D + k]) * bf2f(gw[k]);
    float g = 1.f / (1.f + __expf(-z));
    out[b * D + n] = __float2bfloat16(g * A + (1.f - g) * F);
  }
}

extern "C" void kernel_launch(void* const* d_in, const int* in_sizes, int n_in,
                              void* d_out, int out_size, void* d_ws, size_t ws_size,
                              hipStream_t stream) {
  const unsigned short* x = (const unsigned short*)d_in[0];
  const unsigned short* kc = (const unsigned short*)d_in[1];
  const unsigned short* vc = (const unsigned short*)d_in[2];
  const unsigned short* wq = (const unsigned short*)d_in[3];
  const unsigned short* wo = (const unsigned short*)d_in[4];
  const unsigned short* w1 = (const unsigned short*)d_in[6];
  const unsigned short* w2 = (const unsigned short*)d_in[7];
  const unsigned short* gw = (const unsigned short*)d_in[8];
  char* ws = (char*)d_ws;
  __hip_bfloat16* q = (__hip_bfloat16*)ws;
  float* hbuf = (float*)(ws + 65536);
  float* po = (float*)(ws + 131072);
  float* pml = (float*)(ws + 131072 + 256 * 256 * 4);
  k_pre<<<(H * DH + DFF) / 64, 64, 0, stream>>>(x, wq, w1, q, hbuf);
  k_attn<<<256, 512, 0, stream>>>((const unsigned short*)q, kc, vc, po, pml);
  k_post<<<112, 256, 0, stream>>>(po, pml, hbuf, x, wo, w2, gw, (__hip_bfloat16*)d_out);
}
```

```cpp
#include <hip/hip_runtime.h>
#include <hip/hip_bf16.h>
#include <stdint.h>

typedef unsigned int u32;
typedef __attribute__((ext_vector_type(4))) unsigned u32x4;
typedef __attribute__((ext_vector_type(2))) __bf16 bf16x2;

constexpr int B = 2, D = 448, H = 16, SK = 4096, DH = 256, DFF = 1792;
constexpr int NSPLIT = 8, KEYS = SK / NSPLIT;

__device__ __forceinline__ float bf2f(unsigned short v) { return __uint_as_float(((u32)v) << 16); }
__device__ __forceinline__ float lo2f(u32 v) { return __uint_as_float(v << 16); }
__device__ __forceinline__ float hi2f(u32 v) { return __uint_as_float(v & 0xffff0000u); }
__device__ __forceinline__ float dot2(u32 a, u32 b, float c) {
  return __builtin_amdgcn_fdot2_f32_bf16(__builtin_bit_cast(bf16x2, a), __builtin_bit_cast(bf16x2, b), c, false);
}

__global__ void k_pre(const unsigned short* x, const unsigned short* wq, const unsigned short* w1,
                      __hip_bfloat16* q, float* hbuf) {
  int col = blockIdx.x * 64 + threadIdx.x;
  float a0 = 0.f, a1 = 0.f;
  if (col < H * DH) {
    for (int k = 0; k < D; ++k) { float w = bf2f(wq[k * (H * DH) + col]); a0 += bf2f(x[k]) * w; a1 += bf2f(x[D + k]) * w; }
    q[col] = __float2bfloat16(a0); q[H * DH + col] = __float2bfloat16(a1);
  } else {
    int c = col - H * DH;
    for (int k = 0; k < D; ++k) { float w = bf2f(w1[k * DFF + c]); a0 += bf2f(x[k]) * w; a1 += bf2f(x[D + k]) * w; }
    hbuf[c] = fmaxf(a0, 0.f); hbuf[DFF + c] = fmaxf(a1, 0.f);
  }
}

__global__ void __launch_bounds__(512) k_attn(const unsigned short* q, const unsigned short* kc, const unsigned short* vc,
                                              float* po, float* pml) {
  __shared__ float s_sc[KEYS];
  __shared__ float s_red[8][DH];
  __shared__ float s_m[8];
  const int blk = (blockIdx.x & 7) * 32 + (blockIdx.x >> 3);
  const int bh = blk >> 3, s = blk & 7;
  const int tid = threadIdx.x, lane = tid & 63, wave = tid >> 6;
  const int half = lane >> 5, c = lane & 31;
  const size_t base = ((size_t)bh * SK + s * KEYS + wave * 64) * DH;
  const u32x4* kp = (const u32x4*)(kc + base) + lane;
  const u32x4* vp = (const u32x4*)(vc + base) + lane;
  u32x4 qv = *(const u32x4*)(q + bh * DH + c * 8);
  u32x4 buf[32];
#pragma unroll
  for (int i = 0; i < 32; ++i) buf[i] = __builtin_nontemporal_load(kp + i * 64);
  float wm = -INFINITY;
#pragma unroll
  for (int i = 0; i < 32; ++i) {
    float d = dot2(buf[i][0], qv[0], 0.f); d = dot2(buf[i][1], qv[1], d); d = dot2(buf[i][2], qv[2], d); d = dot2(buf[i][3], qv[3], d);
    buf[i] = __builtin_nontemporal_load(vp + i * 64);
    for (int o = 16; o >= 1; o >>= 1) d += __shfl_xor(d, o);
    d *= 0.0625f;
    if (c == 0) s_sc[wave * 64 + i * 2 + half] = d;
    wm = fmaxf(wm, d);
  }
  wm = fmaxf(wm, __shfl_xor(wm, 32));
  if (lane == 0) s_m[wave] = wm;
  __syncthreads();
  float m = s_m[0];
  for (int w = 1; w < 8; ++w) m = fmaxf(m, s_m[w]);
  float acc[8] = {0, 0, 0, 0, 0, 0, 0, 0};
  float l = 0.f;
#pragma unroll
  for (int i = 0; i < 32; ++i) {
    float p = __expf(s_sc[wave * 64 + i * 2 + half] - m);
    l += p;
#pragma unroll
    for (int j = 0; j < 4; ++j) { acc[2 * j] += p * lo2f(buf[i][j]); acc[2 * j + 1] += p * hi2f(buf[i][j]); }
  }
#pragma unroll
  for (int j = 0; j < 8; ++j) acc[j] += __shfl_xor(acc[j], 32);
  l += __shfl_xor(l, 32);
  if (half == 0) {
#pragma unroll
    for (int j = 0; j < 8; ++j) s_red[wave][c * 8 + j] = acc[j];
  }
  __syncthreads();
  if (lane == 0) s_m[wave] = l;
  __syncthreads();
  if (tid < DH) {
    float o = 0.f;
    for (int w = 0; w < 8; ++w) o += s_red[w][tid];
    po[(size_t)blk * DH + tid] = o;
  }
  if (tid == 0) {
    float L = 0.f;
    for (int w = 0; w < 8; ++w) L += s_m[w];
    pml[blk * 2] = m; pml[blk * 2 + 1] = L;
  }
}

__global__ void __launch_bounds__(256) k_post(const float* po, const float* pml, const float* hbuf, const unsigned short* x,
                       const unsigned short* wo, const unsigned short* w2, const unsigned short* gw, __hip_bfloat16* out) {
  __shared__ float s_o[H * DH];
  __shared__ float s_r[256];
  __shared__ float s_r2[256];
  const int b = blockIdx.x / 56, cg = blockIdx.x % 56, tid = threadIdx.x;
  for (int h = 0; h < H; ++h) {
    int bh = b * H + h;
    float M = -INFINITY;
    for (int s = 0; s < NSPLIT; ++s) M = fmaxf(M, pml[(bh * NSPLIT + s) * 2]);
    float L = 0.f, o = 0.f;
    for (int s = 0; s < NSPLIT; ++s) { float w = __expf(pml[(bh * NSPLIT + s) * 2] - M); L += w * pml[(bh * NSPLIT + s) * 2 + 1]; o += w * po[(size_t)(bh * NSPLIT + s) * DH + tid]; }
    s_o[h * DH + tid] = o / L;
  }
  __syncthreads();
  const int n = cg * 8 + (tid & 7), r = tid >> 3;
  float a = 0.f, f = 0.f;
  for (int k = r; k < H * DH; k += 32) a += s_o[k] * bf2f(wo[k * D + n]);
  for (int k = r; k < DFF; k += 32) f += hbuf[b * DFF + k] * bf2f(w2[k * D + n]);
  s_r[tid] = a; s_r2[tid] = f;
  __syncthreads();
  if (tid < 8) {
    float A = 0.f, F = 0.f;
    for (int j = 0; j < 32; ++j) { A += s_r[j * 8 + tid]; F += s_r2[j * 8 + tid]; }
    float z = 0.f;
    for (int k = 0; k < D; ++k) z += bf2f(x[b * D + k]) * bf2f(gw[k]);
    float g = 1.f / (1.f + __expf(-z));
    out[b * D + n] = __float2bfloat16(g * A + (1.f - g) * F);
  }
}

extern "C" void kernel_launch(void* const* d_in, const int* in_sizes, int n_in,
                              void* d_out, int out_size, void* d_ws, size_t ws_size,
                              hipStream_t stream) {
  const unsigned short* x = (const unsigned short*)d_in[0];
  const unsigned short* kc = (const unsigned short*)d_in[1];
  const unsigned short* vc = (const unsigned short*)d_in[2];
  const unsigned short* wq = (const unsigned short*)d_in[3];
  const unsigned short* wo = (const unsigned short*)d_in[4];
  const unsigned short* w1 = (const unsigned short*)d_in[6];
  const unsigned short* w2 = (const unsigned short*)d_in[7];
  const unsigned short* gw = (const unsigned short*)d_in[8];
  char* ws = (char*)d_ws;
  __hip_bfloat16* q = (__hip_bfloat16*)ws;
  float* hbuf = (float*)(ws + 65536);
  float* po = (float*)(ws + 131072);
  float* pml = (float*)(ws + 131072 + 256 * 256 * 4);
  k_pre<<<(H * DH + DFF) / 64, 64, 0, stream>>>(x, wq, w1, q, hbuf);
  k_attn<<<256, 512, 0, stream>>>((const unsigned short*)q, kc, vc, po, pml);
  k_post<<<112, 256, 0, stream>>>(po, pml, hbuf, x, wo, w2, gw, (__hip_bfloat16*)d_out);
}
```

```cpp
#include <hip/hip_runtime.h>
#include <hip/hip_bf16.h>
#include <stdint.h>

typedef unsigned int u32;
typedef __attribute__((ext_vector_type(4))) unsigned u32x4;
typedef __attribute__((ext_vector_type(2))) unsigned u32x2;
typedef __attribute__((ext_vector_type(4))) float f32x4;
typedef __attribute__((ext_vector_type(2))) float f32x2;
typedef __attribute__((ext_vector_type(2))) __bf16 bf16x2;
typedef __amdgpu_buffer_rsrc_t rsrc_t;

constexpr int D = 448, H = 16, SK = 4096, DH = 256, DFF = 1792;
constexpr int WIN = 32;
constexpr int AUX_NT = 2, AUX_SC1 = 16;
constexpr int AUX_W = 2;
constexpr int PACE = 8;
constexpr int SLEEP1 = 1;
constexpr int SLEEP2 = 1;
constexpr int LAG = 16;
constexpr int PRE = 4;
constexpr int EARLY_PROJ = 0;
constexpr int PROJ_AHEAD = 0;
constexpr int QSLEEP = 40;
constexpr u32 SPIN_CAP = 1u << 20;

constexpr int WS_EPOCH = 0;
constexpr int WS_Q = 4096;
constexpr int WS_H = WS_Q + 32 * 8192;
constexpr int WS_ML = WS_H + 16 * 16384;
constexpr int WS_O = WS_ML + 32 * 1024;
constexpr int WS_Y = WS_O + 16 * 32768;
constexpr int WS_END = WS_Y + 56 * 4096;
constexpr int OOB = 0x40000000;

__device__ __forceinline__ float lo2f(u32 v) { return __uint_as_float(v << 16); }
__device__ __forceinline__ float hi2f(u32 v) { return __uint_as_float(v & 0xffff0000u); }
__device__ __forceinline__ float dot2(u32 a, u32 b, float c) {
  return __builtin_amdgcn_fdot2_f32_bf16(__builtin_bit_cast(bf16x2, a), __builtin_bit_cast(bf16x2, b), c, false);
}
__device__ __forceinline__ u32 pack_bf16(float lo, float hi) {
  bf16x2 r; r[0] = (__bf16)lo; r[1] = (__bf16)hi; return __builtin_bit_cast(u32, r);
}
template <int CTRL> __device__ __forceinline__ float dpp(float x) {
  return __builtin_bit_cast(float, __builtin_amdgcn_mov_dpp(__builtin_bit_cast(int, x), CTRL, 0xf, 0xf, true));
}
constexpr int XOR1 = 0xB1, XOR2 = 0x4E, XOR7 = 0x141, XOR8 = 0x128, ROR4 = 0x124;
__device__ __forceinline__ float merge32(float a, float b) {
  auto s = __builtin_amdgcn_permlane32_swap(__float_as_uint(a), __float_as_uint(b), false, false);
  return __uint_as_float(s[0]) + __uint_as_float(s[1]);
}
__device__ __forceinline__ float merge16(float a, float b) {
  auto s = __builtin_amdgcn_permlane16_swap(__float_as_uint(a), __float_as_uint(b), false, false);
  return __uint_as_float(s[0]) + __uint_as_float(s[1]);
}
template <int CTRL> __device__ __forceinline__ float merged(float a, float b, bool sel) {
  const float t1 = a + dpp<CTRL>(a), t2 = b + dpp<CTRL>(b);
  return sel ? t2 : t1;
}
__device__ __forceinline__ float max32(float a) {
  auto s = __builtin_amdgcn_permlane32_swap(__float_as_uint(a), __float_as_uint(a), false, false);
  return fmaxf(__uint_as_float(s[0]), __uint_as_float(s[1]));
}
__device__ __forceinline__ float max16(float a) {
  auto s = __builtin_amdgcn_permlane16_swap(__float_as_uint(a), __float_as_uint(a), false, false);
  return fmaxf(__uint_as_float(s[0]), __uint_as_float(s[1]));
}
__device__ __forceinline__ float wave_max(float x) {
  x = fmaxf(x, dpp<XOR1>(x)); x = fmaxf(x, dpp<XOR2>(x)); x = fmaxf(x, dpp<XOR7>(x)); x = fmaxf(x, dpp<XOR8>(x));
  return max32(max16(x));
}
__device__ __forceinline__ float wave_sum(float x) {
  x += dpp<XOR1>(x); x += dpp<XOR2>(x); x += dpp<XOR7>(x); x += dpp<XOR8>(x);
  x = merge16(x, x); return merge32(x, x);
}
__device__ __forceinline__ rsrc_t mkrsrc(const void* p, u32 bytes) {
  return __builtin_amdgcn_make_buffer_rsrc((void*)p, 0, bytes, 0x00020000);
}
template <int AUX> __device__ __forceinline__ u32x4 ld16(rsrc_t r, int voff, int soff) {
  return __builtin_bit_cast(u32x4, __builtin_amdgcn_raw_buffer_load_b128(r, voff, soff, AUX));
}
__device__ __forceinline__ u32 ldu16(rsrc_t r, int voff) { return (u32)__builtin_amdgcn_raw_buffer_load_b16(r, voff, 0, 0); }
__device__ __forceinline__ void st16_sc1(rsrc_t r, int voff, int soff, u32x4 v) {
  __builtin_amdgcn_raw_buffer_store_b128(v, r, voff, soff, AUX_SC1);
}
__device__ __forceinline__ void wg_barrier() {
  asm volatile("s_waitcnt lgkmcnt(0)\n\ts_barrier" ::: "memory");
}
#define SCHED_FENCE() __builtin_amdgcn_sched_barrier(0)

template <int N, int SLEEP> __device__ __forceinline__ void gather(rsrc_t r, int voff, int soff, int stride, u32 tag, u32x4 (&g)[N]) {
  bool ok[N];
#pragma unroll
  for (int k = 0; k < N; ++k) g[k] = ld16<AUX_SC1>(r, voff, soff + k * stride);
#pragma nounroll
  for (u32 spin = 0; spin < SPIN_CAP; ++spin) {
    bool all = true;
#pragma unroll
    for (int k = 0; k < N; ++k) { ok[k] = g[k][1] == tag && g[k][3] == tag; all = all && ok[k]; }
    if (__all(all)) break;
    asm volatile("s_sleep %0" :: "i"(SLEEP) : "memory");
    u32x4 t[N];
#pragma unroll
    for (int k = 0; k < N; ++k) t[k] = ld16<AUX_SC1>(r, ok[k] ? OOB : voff, soff + k * stride);
#pragma unroll
    for (int k = 0; k < N; ++k) g[k] = ok[k] ? g[k] : t[k];
  }
}

template <int N> __device__ __forceinline__ f32x2 xw_partial(const u32x4 (&w)[N], const u32 (&x0)[N], const u32 (&x1)[N], int lane) {
  float a[16];
#pragma unroll
  for (int k = 0; k < 16; ++k) a[k] = 0.f;
#pragma unroll
  for (int j = 0; j < N; ++j) {
    const float xa = (lane & 8) ? hi2f(x0[j]) : lo2f(x0[j]), xb = (lane & 8) ? hi2f(x1[j]) : lo2f(x1[j]);
#pragma unroll
    for (int k = 0; k < 4; ++k) {
      const float lo = lo2f(w[j][k]), hi = hi2f(w[j][k]);
      a[2 * k] = fmaf(xa, lo, a[2 * k]); a[2 * k + 1] = fmaf(xa, hi, a[2 * k + 1]);
      a[8 + 2 * k] = fmaf(xb, lo, a[8 + 2 * k]); a[9 + 2 * k] = fmaf(xb, hi, a[9 + 2 * k]);
    }
  }
#pragma unroll
  for (int k = 0; k < 8; ++k) a[k] = merge32(a[k], a[k + 8]);
#pragma unroll
  for (int k = 0; k < 4; ++k) a[k] = merge16(a[k], a[k + 4]);
  return f32x2{merged<XOR8>(a[0], a[2], lane & 8), merged<XOR8>(a[1], a[3], lane & 8)};
}

__global__ void __launch_bounds__(512, 2)
fused_kernel(const unsigned short* __restrict__ x, const unsigned short* __restrict__ kc, const unsigned short* __restrict__ vc,
             const unsigned short* __restrict__ wq, const unsigned short* __restrict__ wo, const unsigned short* __restrict__ w1,
             const unsigned short* __restrict__ w2, const unsigned short* __restrict__ gw, unsigned short* __restrict__ out,
             unsigned char* ws) {
  __shared__ __attribute__((aligned(16))) float s_sc[8][68];
  __shared__ __attribute__((aligned(16))) float s_y[8][128];
  __shared__ __attribute__((aligned(16))) u32 s_q[128];
  __shared__ __attribute__((aligned(16))) float s_qp[4][128];
  __shared__ __attribute__((aligned(16))) float s_h[128];
  __shared__ __attribute__((aligned(16))) float s_m[4];
  __shared__ __attribute__((aligned(16))) float s_l[4];
  __shared__ __attribute__((aligned(16))) float s_g[4];
  __shared__ __attribute__((aligned(16))) u32 s_cnt[4];

  const int tid = threadIdx.x, lane = tid & 63;
  const int wave = __builtin_amdgcn_readfirstlane(tid >> 6);
  const int xcd = blockIdx.x & 7, r = blockIdx.x >> 3;
  const int h = 2 * xcd + (r >> 4), mem = r & 15, b = mem >> 3, s = mem & 7;
  const int bh = b * H + h;
  const int rh = mem >= 7 ? 1 : 0, cb = mem - 7 * rh;
  const rsrc_t rws = mkrsrc(ws, WS_END);

  const int g8 = lane & 7, c8 = lane >> 3;
  u32x4 wov[2], w2v;
  u32 tag;
  u32 epoch;
  asm volatile("" :: "s"(x), "s"(kc), "s"(vc), "s"(wq), "s"(wo), "s"(w1), "s"(w2), "s"(gw), "s"(out), "s"(ws));
  auto load_proj = [&]() {
    const rsrc_t rwo = mkrsrc(wo + (size_t)(h * DH + rh * 128 + wave * 16) * D, mem < 14 ? 16 * D * 2 : 0);
    const rsrc_t rw2 = mkrsrc(w2 + (size_t)(h < 14 ? h * 128 + rh * 64 + wave * 8 : 0) * D, mem < 14 && h < 14 ? 8 * D * 2 : 0);
#pragma unroll
    for (int j = 0; j < 2; ++j) wov[j] = ld16<AUX_W>(rwo, (2 * g8 + j) * (D * 2) + cb * 128 + c8 * 16, 0);
    w2v = ld16<AUX_W>(rw2, g8 * (D * 2) + cb * 128 + c8 * 16, 0);
  };
  float yac[16], wf[2][8];
  auto prep_proj = [&]() {
    const float h0 = s_h[wave * 8 + g8], h1 = s_h[64 + wave * 8 + g8];
#pragma unroll
    for (int k = 0; k < 4; ++k) {
      yac[2 * k] = h0 * lo2f(w2v[k]); yac[2 * k + 1] = h0 * hi2f(w2v[k]);
      yac[8 + 2 * k] = h1 * lo2f(w2v[k]); yac[9 + 2 * k] = h1 * hi2f(w2v[k]);
    }
#pragma unroll
    for (int j = 0; j < 2; ++j)
#pragma unroll
      for (int k = 0; k < 4; ++k) { wf[j][2 * k] = lo2f(wov[j][k]); wf[j][2 * k + 1] = hi2f(wov[j][k]); }
#pragma unroll
    for (int k = 0; k < 8; ++k) asm volatile("" : "+v"(wf[0][k]), "+v"(wf[1][k]), "+v"(yac[k]), "+v"(yac[8 + k]));
  };

  if (wave >= 4) {
    const int sw = wave - 4;
    const size_t base = ((size_t)bh * SK + s * 512) * DH;
    const rsrc_t rk = mkrsrc(kc + base, 262144), rv = mkrsrc(vc + base, 262144);
    const int koff = (tid - 256) * 16;
    const int voff = c8 * 512 + sw * 128 + g8 * 16;
    u32x4 buf[WIN];
    auto issue = [&](int n) {
      if (n < 128) {
        if (n >= PACE) asm volatile("s_waitcnt vmcnt(%0)" :: "i"(PACE - 1) : "memory");
        buf[n & 31] = n < 64 ? ld16<AUX_NT>(rk, koff, n * 4096) : ld16<AUX_NT>(rv, voff, (n - 64) * 4096);
      }
    };
#pragma unroll
    for (int i = 0; i < PRE; ++i) { issue(i); SCHED_FENCE(); }
    asm volatile("s_barrier" ::: "memory");
    asm volatile("s_load_dword %0, %1, 0x0" : "=s"(epoch) : "s"(ws) : "memory");
#pragma unroll
    for (int i = PRE; i < WIN; ++i) { issue(i); SCHED_FENCE(); }
    wg_barrier();
    asm volatile("" : "+s"(epoch));
    tag = epoch + 1u;
    const u32x4 qv = *(const u32x4*)&s_q[(lane & 31) * 4];
    float sc[2];
#pragma unroll
    for (int blk = 0; blk < 2; ++blk) {
      float lv[5];
#pragma unroll
      for (int i = 0; i < 32; ++i) {
        const u32x4 kv = buf[i];
        float d = dot2(kv[0], qv[0], 0.f); d = dot2(kv[1], qv[1], d); d = dot2(kv[2], qv[2], d); d = dot2(kv[3], qv[3], d);
        asm volatile("" : "+v"(d));
        SCHED_FENCE();
        if (blk == 0) { if ((i + 1) * LAG / 32 != i * LAG / 32) issue(32 + i * LAG / 32); }
        else issue(32 + i + LAG);
        SCHED_FENCE();
        if (i & 1) {
          d = merge16(lv[0], d);
          if (i & 2) {
            d = merged<XOR8>(lv[1], d, lane & 8);
            if (i & 4) {
              d = merged<XOR7>(lv[2], d, lane & 4);
              if (i & 8) {
                d = merged<XOR2>(lv[3], d, lane & 2);
                if (i & 16) sc[blk] = merged<XOR1>(lv[4], d, lane & 1);
                else lv[4] = d;
              } else lv[3] = d;
            } else lv[2] = d;
          } else lv[1] = d;
        } else lv[0] = d;
      }
    }
    constexpr float C2 = 0.0625f * 1.4426950408889634f;
    sc[0] *= C2; sc[1] *= C2;
    const int ir = __builtin_bitreverse32((u32)(lane & 31)) >> 27;
    s_sc[2 * sw + (lane >> 5)][ir] = sc[0];
    s_sc[2 * sw + (lane >> 5)][32 + ir] = sc[1];
    const float m = wave_max(fmaxf(sc[0], sc[1]));
    const float l = wave_sum(__builtin_amdgcn_exp2f(sc[0] - m) + __builtin_amdgcn_exp2f(sc[1] - m));
    if (lane == 0) { s_m[sw] = m; s_l[sw] = l; }
    wg_barrier();
    const f32x4 mv = *(const f32x4*)s_m;
    const float M = fmaxf(fmaxf(mv[0], mv[1]), fmaxf(mv[2], mv[3]));
    float acc[8], F;
    int ooff = (lane & 8) ? OOB : (g8 >> 1) * 2048 + (4 * (g8 & 1) + 2 * (lane >> 5) + ((lane >> 4) & 1)) * 16;
#pragma unroll
    for (int j = 0; j < 8; ++j) acc[j] = 0.f;
#pragma unroll
    for (int i4 = 0; i4 < 16; ++i4) {
      const f32x4 sv = *(const f32x4*)&s_sc[c8][i4 * 4];
#pragma unroll
      for (int ii = 0; ii < 4; ++ii) {
        const int i = i4 * 4 + ii;
        const float p = __builtin_amdgcn_exp2f(sv[ii] - M);
        const u32x4 vv = buf[i & 31];
#pragma unroll
        for (int j = 0; j < 4; ++j) { acc[2 * j] = fmaf(p, lo2f(vv[j]), acc[2 * j]); acc[2 * j + 1] = fmaf(p, hi2f(vv[j]), acc[2 * j + 1]); }
#pragma unroll
        for (int j = 0; j < 8; ++j) asm volatile("" : "+v"(acc[j]));
        SCHED_FENCE();
        issue(64 + i + LAG);
        if (64 + i + LAG == 127 - PROJ_AHEAD) {
          asm volatile("s_barrier" ::: "memory");
          load_proj();
          F = s_g[2];
          asm volatile("" : "+v"(F), "+v"(ooff));
        }
        SCHED_FENCE();
      }
    }
    float t[4], u[2];
#pragma unroll
    for (int k = 0; k < 4; ++k) t[k] = merge32(acc[k], acc[k + 4]);
#pragma unroll
    for (int k = 0; k < 2; ++k) { u[k] = merge16(t[k], t[k + 2]); u[k] = (u[k] + dpp<XOR8>(u[k])) * F; }
    st16_sc1(rws, ooff, WS_O + h * 32768 + sw * 8192 + mem * 128, u32x4{__float_as_uint(u[0]), tag, __float_as_uint(u[1]), tag});
    SCHED_FENCE();
    prep_proj();
  } else {
    const int hw = wave;
    __builtin_amdgcn_s_setprio(3);
    const rsrc_t rx = mkrsrc(x, 2 * D * 2);
    {
      const rsrc_t rwq = mkrsrc(wq + (size_t)(mem >> 2) * 112 * (H * DH), 112 * H * DH * 2);
      u32 x0[4], x1[4];
      u32x4 wv[4];
#pragma unroll
      for (int j = 0; j < 4; ++j) wv[j] = ld16<AUX_W>(rwq, (hw * 32 + j * 8 + c8) * (H * DH * 2) + h * 512 + (mem & 3) * 128 + g8 * 16, 0);
#pragma unroll
      for (int j = 0; j < 4; ++j) {
        const int xo = ((mem >> 2) * 112 + min(hw * 32 + j * 8 + (c8 & 6), 110)) * 2;
        x0[j] = __builtin_amdgcn_raw_buffer_load_b32(rx, xo, 0, 0);
        x1[j] = __builtin_amdgcn_raw_buffer_load_b32(rx, xo, D * 2, 0);
      }
      if (tid == 0) s_cnt[0] = 0;
      SCHED_FENCE();
      wg_barrier();
      asm volatile("s_load_dword %0, %1, 0x0" : "=s"(epoch) : "s"(ws) : "memory");
#pragma unroll
      for (int j = 0; j < 4; ++j) asm volatile("" : "+v"(wv[j]), "+v"(x0[j]), "+v"(x1[j]));
      *(f32x2*)&s_qp[hw][lane * 2] = xw_partial<4>(wv, x0, x1, lane);
    }
    asm volatile("s_waitcnt lgkmcnt(0)" : "+s"(epoch) :: "memory");
    tag = epoch + 1u;
    const int poff = (lane >> 5) * 512 + g8 * 64 + ((lane >> 3) & 3) * 16;
    if (hw != 0) {
      if (lane == 0) __hip_atomic_fetch_add(&s_cnt[0], 1u, __ATOMIC_RELAXED, __HIP_MEMORY_SCOPE_WORKGROUP);
    } else {
#pragma nounroll
      while (__hip_atomic_load(&s_cnt[0], __ATOMIC_RELAXED, __HIP_MEMORY_SCOPE_WORKGROUP) != 3u) {}
      asm volatile("" ::: "memory");
      f32x2 q2 = *(const f32x2*)&s_qp[0][lane * 2];
#pragma unroll
      for (int w = 1; w < 4; ++w) q2 += *(const f32x2*)&s_qp[w][lane * 2];
      st16_sc1(rws, (lane >> 5) * 8192 + (poff & 511), WS_Q + h * 16384 + (mem >> 2) * 2048 + (mem & 3) * 512,
               u32x4{__float_as_uint(q2[0]), tag, __float_as_uint(q2[1]), tag});
    }
    if (hw == 1 || hw == 2) {
      asm volatile("s_sleep %0" :: "i"(QSLEEP) : "memory");
      u32x4 gq[4];
      gather<4, 1>(rws, lane * 16, WS_Q + h * 16384 + b * 8192 + (hw - 1) * 1024, 2048, tag, gq);
      s_q[(hw - 1) * 64 + lane] = pack_bf16((__uint_as_float(gq[0][0]) + __uint_as_float(gq[1][0])) + (__uint_as_float(gq[2][0]) + __uint_as_float(gq[3][0])),
                                            (__uint_as_float(gq[0][2]) + __uint_as_float(gq[1][2])) + (__uint_as_float(gq[2][2]) + __uint_as_float(gq[3][2])));
    }
    wg_barrier();
    if (hw == 3) {
      const rsrc_t rw1 = mkrsrc(w1 + (size_t)(mem >> 1) * 56 * DFF, h < 14 ? 56 * DFF * 2 : 0);
      u32 x0[7], x1[7];
      u32x4 wv[7];
#pragma unroll
      for (int j = 0; j < 7; ++j) {
        wv[j] = ld16<AUX_W>(rw1, (j * 8 + c8) * (DFF * 2) + h * 256 + (mem & 1) * 128 + g8 * 16, 0);
        const int xo = ((mem >> 1) * 56 + j * 8 + (c8 & 6)) * 2;
        x0[j] = __builtin_amdgcn_raw_buffer_load_b32(rx, xo, 0, 0);
        x1[j] = __builtin_amdgcn_raw_buffer_load_b32(rx, xo, D * 2, 0);
      }
      const f32x2 hp = xw_partial<7>(wv, x0, x1, lane);
      st16_sc1(rws, poff, WS_H + h * 16384 + (mem & 1) * 8192 + (mem >> 1) * 1024, u32x4{__float_as_uint(hp[0]), tag, __float_as_uint(hp[1]), tag});
    } else if (hw == 1 || hw == 2) {
      const rsrc_t rg = mkrsrc(gw, D * 2);
      const u32x4 xa = ld16<0>(rx, lane < 56 ? lane * 16 : OOB, (hw - 1) * D * 2), ga = ld16<0>(rg, lane * 16, 0);
      float z = dot2(xa[0], ga[0], 0.f); z = dot2(xa[1], ga[1], z); z = dot2(xa[2], ga[2], z); z = dot2(xa[3], ga[3], z);
      z = wave_sum(z);
      if (lane == 0) s_g[hw - 1] = 1.f / (1.f + __expf(-z));
    }
    wg_barrier();
    if (hw == 0) {
      const f32x4 mv = *(const f32x4*)s_m, lv = *(const f32x4*)s_l;
      const float M = fmaxf(fmaxf(mv[0], mv[1]), fmaxf(mv[2], mv[3]));
      float L = 0.f;
#pragma unroll
      for (int w = 0; w < 4; ++w) L = fmaf(__builtin_amdgcn_exp2f(mv[w] - M), lv[w], L);
      st16_sc1(rws, lane < 8 ? lane * 16 : OOB, WS_ML + bh * 1024 + s * 128, u32x4{__float_as_uint(M), tag, __float_as_uint(L), tag});
      asm volatile("s_sleep 16" ::: "memory");
      u32x4 gm[1];
      gather<1, 8>(rws, lane * 16, WS_ML + bh * 1024, 0, tag, gm);
      const float ms = __uint_as_float(gm[0][0]);
      const float MM = max32(max16(fmaxf(ms, dpp<XOR8>(ms))));
      float LL = __builtin_amdgcn_exp2f(ms - MM) * __uint_as_float(gm[0][2]);
      LL += dpp<XOR8>(LL); LL = merge16(LL, LL); LL = merge32(LL, LL);
      if (lane == 0) s_g[2] = s_g[b] * __builtin_amdgcn_exp2f(M - MM) / LL;
    } else if (hw == 3) {
      u32x4 gh[8];
      gather<8, 8>(rws, lane * 16, WS_H + h * 16384 + rh * 8192, 1024, tag, gh);
      f32x2 hs = {0.f, 0.f};
#pragma unroll
      for (int k = 0; k < 8; ++k) hs += f32x2{__uint_as_float(gh[k][0]), __uint_as_float(gh[k][2])};
      const float ng = 1.f - s_g[lane >> 5];
      *(f32x2*)&s_h[lane * 2] = f32x2{fmaxf(hs[0], 0.f) * ng, fmaxf(hs[1], 0.f) * ng};
    }
    wg_barrier();
    load_proj();
    prep_proj();
  }

  if (mem < 14) {
    u32x4 go[2];
    gather<2, SLEEP1>(rws, lane * 16, WS_O + h * 32768 + (rh * 8 + wave) * 2048, 1024, tag, go);
    float o[4] = {__uint_as_float(go[0][0]), __uint_as_float(go[0][2]), __uint_as_float(go[1][0]), __uint_as_float(go[1][2])};
#pragma unroll
    for (int j = 0; j < 4; ++j) { o[j] += dpp<XOR8>(o[j]); o[j] = merge16(o[j], o[j]); o[j] = merge32(o[j], o[j]); }
#pragma unroll
    for (int k = 0; k < 8; ++k) {
      yac[k] = fmaf(o[1], wf[1][k], fmaf(o[0], wf[0][k], yac[k]));
      yac[8 + k] = fmaf(o[3], wf[1][k], fmaf(o[2], wf[0][k], yac[8 + k]));
    }
#pragma unroll
    for (int k = 0; k < 8; ++k) yac[k] = merged<XOR7>(yac[k], yac[k + 8], lane & 4);
#pragma unroll
    for (int k = 0; k < 4; ++k) yac[k] = merged<XOR2>(yac[k], yac[k + 4], lane & 2);
    *(f32x2*)&s_y[wave][((lane >> 2) & 1) * 64 + c8 * 8 + (lane & 3) * 2] = f32x2{merged<XOR1>(yac[0], yac[2], lane & 1), merged<XOR1>(yac[1], yac[3], lane & 1)};
    wg_barrier();
    if (wave == 0) {
      f32x2 y = *(const f32x2*)&s_y[0][lane * 2];
#pragma unroll
      for (int w = 1; w < 8; ++w) y += *(const f32x2*)&s_y[w][lane * 2];
      st16_sc1(rws, ((lane >> 5) * 28 + ((lane >> 3) & 3)) * 4096 + g8 * 16, WS_Y + cb * 16384 + (h * 2 + rh) * 128,
               u32x4{__float_as_uint(y[0]), tag, __float_as_uint(y[1]), tag});
    }
  } else {
   asm volatile("s_barrier" ::: "memory");
   if (wave < 2 && h * 4 + (mem & 1) * 2 + wave < 56) {
    const int cq = h * 4 + (mem & 1) * 2 + wave;
    u32x4 gy[4];
    gather<4, SLEEP2>(rws, lane * 16, WS_Y + cq * 4096, 1024, tag, gy);
    float y0 = (__uint_as_float(gy[0][0]) + __uint_as_float(gy[1][0])) + (__uint_as_float(gy[2][0]) + __uint_as_float(gy[3][0]));
    float y1 = (__uint_as_float(gy[0][2]) + __uint_as_float(gy[1][2])) + (__uint_as_float(gy[2][2]) + __uint_as_float(gy[3][2]));
    y0 += dpp<XOR8>(y0); y0 = merge16(y0, y0); y0 = merge32(y0, y0);
    y1 += dpp<XOR8>(y1); y1 = merge16(y1, y1); y1 = merge32(y1, y1);
    if (lane < 8) ((u32*)out)[cq * 8 + lane] = pack_bf16(y0, y1);
    if (cq == 0 && lane == 0) __hip_atomic_store((u32*)(ws + WS_EPOCH), tag, __ATOMIC_RELAXED, __HIP_MEMORY_SCOPE_AGENT);
   }
  }
}

extern "C" void kernel_launch(void* const* d_in, const int* in_sizes, int n_in,
                              void* d_out, int out_size, void* d_ws, size_t ws_size,
                              hipStream_t stream) {
  fused_kernel<<<256, 512, 0, stream>>>((const unsigned short*)d_in[0], (const unsigned short*)d_in[1], (const unsigned short*)d_in[2],
                                        (const unsigned short*)d_in[3], (const unsigned short*)d_in[4], (const unsigned short*)d_in[6],
                                        (const unsigned short*)d_in[7], (const unsigned short*)d_in[8], (unsigned short*)d_out,
                                        (unsigned char*)d_ws);
}
```

```cpp
#include <hip/hip_runtime.h>
#include <hip/hip_bf16.h>
#include <stdint.h>

typedef unsigned int u32;
typedef __attribute__((ext_vector_type(4))) unsigned u32x4;
typedef __attribute__((ext_vector_type(2))) unsigned u32x2;
typedef __attribute__((ext_vector_type(4))) float f32x4;
typedef __attribute__((ext_vector_type(2))) float f32x2;
typedef __attribute__((ext_vector_type(2))) __bf16 bf16x2;
typedef __attribute__((ext_vector_type(8))) __bf16 bf16x8;
typedef __attribute__((ext_vector_type(4))) short s16x4;
typedef __attribute__((ext_vector_type(4))) unsigned long long u64x4;
typedef __attribute__((ext_vector_type(2))) unsigned long long u64x2;
typedef __amdgpu_buffer_rsrc_t rsrc_t;

constexpr int D = 448, H = 16, SK = 4096, DH = 256, DFF = 1792;
constexpr int WIN = 32;
constexpr int AUX_NT = 2, AUX_SC1 = 16;
constexpr int PRE = 6;
constexpr int LAG = 16;
constexpr int CODESH = 16;
constexpr u32 SPIN_CAP = 1u << 20;

constexpr int TH_A = 512;
constexpr int TH_D = 1465, TH_D2 = 1670;
constexpr int KNEE = 107;
constexpr int sched(int n) { return TH_A + (((n < KNEE ? n : KNEE) - 32) * TH_D + (n > KNEE ? n - KNEE : 0) * TH_D2) / 100; }
constexpr int W0 = 6, WSTEP = 30, WLEVELS = 4;
constexpr int WMAX0 = 10, WMAX = 12;
constexpr int T_ML = sched(78) + 310;
constexpr int T_PROJ = sched(114);
constexpr int T_O = sched(127) + 114, T_Y = sched(127) + 211;
constexpr int vload(int i) { return 64 + LAG + i * 48 / (48 + LAG - WMAX); }
constexpr int VLAST = 63 - WMAX;
static_assert(vload(VLAST) == 127 && vload(VLAST - 1) == 126);
constexpr int block_of(int m) { return m * 32768 + (m & 7) * 4096; }

constexpr int WS_EPOCH = 0;
constexpr int WS_T0 = 2048;
constexpr int WS_Q = 4096;
constexpr int WS_H = WS_Q + 32 * 8192;
constexpr int WS_ML = WS_H + 16 * 16384;
constexpr int WS_O = WS_ML + 32 * 1024;
constexpr int WS_Y = WS_O + 16 * 32768;
constexpr int WS_END = WS_Y + 56 * 4096;
constexpr int OOB = 0x40000000;

__device__ __forceinline__ float lo2f(u32 v) { return __uint_as_float(v << 16); }
__device__ __forceinline__ float hi2f(u32 v) { return __uint_as_float(v & 0xffff0000u); }
__device__ __forceinline__ float dot2(u32 a, u32 b, float c) {
  return __builtin_amdgcn_fdot2_f32_bf16(__builtin_bit_cast(bf16x2, a), __builtin_bit_cast(bf16x2, b), c, false);
}
__device__ __forceinline__ u32 pack_bf16(float lo, float hi) {
  bf16x2 r; r[0] = (__bf16)lo; r[1] = (__bf16)hi; return __builtin_bit_cast(u32, r);
}
template <int CTRL> __device__ __forceinline__ float dpp(float x) {
  return __builtin_bit_cast(float, __builtin_amdgcn_mov_dpp(__builtin_bit_cast(int, x), CTRL, 0xf, 0xf, true));
}
constexpr int XOR1 = 0xB1, XOR2 = 0x4E, XOR7 = 0x141, XOR8 = 0x128;
__device__ __forceinline__ float merge32(float a, float b) {
  auto s = __builtin_amdgcn_permlane32_swap(__float_as_uint(a), __float_as_uint(b), false, false);
  return __uint_as_float(s[0]) + __uint_as_float(s[1]);
}
__device__ __forceinline__ float merge16(float a, float b) {
  auto s = __builtin_amdgcn_permlane16_swap(__float_as_uint(a), __float_as_uint(b), false, false);
  return __uint_as_float(s[0]) + __uint_as_float(s[1]);
}
template <int CTRL> __device__ __forceinline__ float merged(float a, float b, bool sel) {
  const float t1 = a + dpp<CTRL>(a), t2 = b + dpp<CTRL>(b);
  return sel ? t2 : t1;
}
__device__ __forceinline__ float max32(float a) {
  auto s = __builtin_amdgcn_permlane32_swap(__float_as_uint(a), __float_as_uint(a), false, false);
  return fmaxf(__uint_as_float(s[0]), __uint_as_float(s[1]));
}
__device__ __forceinline__ float max16(float a) {
  auto s = __builtin_amdgcn_permlane16_swap(__float_as_uint(a), __float_as_uint(a), false, false);
  return fmaxf(__uint_as_float(s[0]), __uint_as_float(s[1]));
}
__device__ __forceinline__ float wave_max(float x) {
  x = fmaxf(x, dpp<XOR1>(x)); x = fmaxf(x, dpp<XOR2>(x)); x = fmaxf(x, dpp<XOR7>(x)); x = fmaxf(x, dpp<XOR8>(x));
  return max32(max16(x));
}
__device__ __forceinline__ float wave_sum(float x) {
  x += dpp<XOR1>(x); x += dpp<XOR2>(x); x += dpp<XOR7>(x); x += dpp<XOR8>(x);
  x = merge16(x, x); return merge32(x, x);
}
__device__ __forceinline__ rsrc_t mkrsrc(const void* p, u32 bytes) {
  return __builtin_amdgcn_make_buffer_rsrc((void*)p, 0, bytes, 0x00020000);
}
template <int AUX> __device__ __forceinline__ u32x4 ld16(rsrc_t r, int voff, int soff) {
  return __builtin_bit_cast(u32x4, __builtin_amdgcn_raw_buffer_load_b128(r, voff, soff, AUX));
}
template <int AUX> __device__ __forceinline__ u32x2 ld8(rsrc_t r, int voff, int soff) {
  return __builtin_bit_cast(u32x2, __builtin_amdgcn_raw_buffer_load_b64(r, voff, soff, AUX));
}
__device__ __forceinline__ void st16_sc1(rsrc_t r, int voff, int soff, u32x4 v) {
  __builtin_amdgcn_raw_buffer_store_b128(v, r, voff, soff, AUX_SC1);
}
__device__ __forceinline__ void wg_barrier() {
  asm volatile("s_waitcnt lgkmcnt(0)\n\ts_barrier" ::: "memory");
}
#define SCHED_FENCE() __builtin_amdgcn_sched_barrier(0)

template <int T> __device__ __forceinline__ void wait_until(u32 t0) {
  asm volatile("1:\n\ts_memrealtime vcc\n\ts_waitcnt lgkmcnt(0)\n\ts_sub_u32 vcc_lo, vcc_lo, %0\n\ts_cmpk_lt_u32 vcc_lo, %1\n\ts_cbranch_scc1 1b"
               :: "s"(t0), "i"(T) : "vcc", "scc", "memory");
}
template <int N> __device__ __forceinline__ void gather_issue(rsrc_t r, int voff, int soff, int stride, u32x4 (&g)[N]) {
#pragma unroll
  for (int k = 0; k < N; ++k) g[k] = ld16<AUX_SC1>(r, voff, soff + k * stride);
}
template <int N, int SLEEP> __device__ __forceinline__ void gather_wait(rsrc_t r, int voff, int soff, int stride, u32 tag, u32x4 (&g)[N]) {
  bool ok[N];
#pragma nounroll
  for (u32 spin = 0; spin < SPIN_CAP; ++spin) {
    bool all = true;
#pragma unroll
    for (int k = 0; k < N; ++k) { ok[k] = g[k][1] == tag && g[k][3] == tag; all = all && ok[k]; }
    if (__all(all)) break;
    asm volatile("s_sleep %0" :: "i"(SLEEP) : "memory");
    u32x4 t[N];
#pragma unroll
    for (int k = 0; k < N; ++k) t[k] = ld16<AUX_SC1>(r, ok[k] ? OOB : voff, soff + k * stride);
#pragma unroll
    for (int k = 0; k < N; ++k) g[k] = ok[k] ? g[k] : t[k];
  }
}

template <int N, int GAP, int ID> __device__ __forceinline__ void gather_wait3(rsrc_t r, int voff, int soff, u32 tag, const u32x4 (&g)[N], u32 (&v)[N][2]) {
  unsigned long long ok[N], all = ~0ull, t;
  u32 spin = SPIN_CAP, a;
#pragma unroll
  for (int k = 0; k < N; ++k) { v[k][0] = g[k][0]; v[k][1] = g[k][2]; ok[k] = __ballot(g[k][1] == tag && g[k][3] == tag); all &= ok[k]; }
  if (__builtin_expect(all == ~0ull, 1)) return;
  if constexpr (N == 2) {
    asm volatile("v_cndmask_b32 %8, %9, 2.0, %4\n\tbuffer_load_dwordx4 v[168:171], %8, %10, %11 offen offset:0 sc1\n\tv_cndmask_b32 %8, %9, 2.0, %5\n\t"
                 "buffer_load_dwordx4 v[172:175], %8, %10, %11 offen offset:1024 sc1\n\ts_sleep %13\n\tv_cndmask_b32 %8, %9, 2.0, %4\n\t"
                 "buffer_load_dwordx4 v[176:179], %8, %10, %11 offen offset:0 sc1\n\tv_cndmask_b32 %8, %9, 2.0, %5\n\t"
                 "buffer_load_dwordx4 v[180:183], %8, %10, %11 offen offset:1024 sc1\n\ts_sleep %13\n\t1:\n\tv_cndmask_b32 %8, %9, 2.0, %4\n\t"
                 "buffer_load_dwordx4 v[184:187], %8, %10, %11 offen offset:0 sc1\n\tv_cndmask_b32 %8, %9, 2.0, %5\n\t"
                 "buffer_load_dwordx4 v[188:191], %8, %10, %11 offen offset:1024 sc1\n\ts_waitcnt vmcnt(4)\n\tv_cmp_eq_u32 vcc, %12, v169\n\tv_cmp_eq_u32 %6, %12, v171\n\t"
                 "s_and_b64 %6, %6, vcc\n\ts_andn2_b64 %6, %6, %4\n\tv_cndmask_b32 %0, %0, v168, %6\n\tv_cndmask_b32 %1, %1, v170, %6\n\ts_or_b64 %4, %4, %6\n\t"
                 "v_cmp_eq_u32 vcc, %12, v173\n\tv_cmp_eq_u32 %6, %12, v175\n\ts_and_b64 %6, %6, vcc\n\ts_andn2_b64 %6, %6, %5\n\tv_cndmask_b32 %2, %2, v172, %6\n\t"
                 "v_cndmask_b32 %3, %3, v174, %6\n\ts_or_b64 %5, %5, %6\n\ts_and_b64 %6, %4, %5\n\ts_cmp_eq_u64 %6, -1\n\ts_cbranch_scc1 2f\n\t"
                 "v_cndmask_b32 %8, %9, 2.0, %4\n\tbuffer_load_dwordx4 v[168:171], %8, %10, %11 offen offset:0 sc1\n\tv_cndmask_b32 %8, %9, 2.0, %5\n\t"
                 "buffer_load_dwordx4 v[172:175], %8, %10, %11 offen offset:1024 sc1\n\ts_waitcnt vmcnt(4)\n\tv_cmp_eq_u32 vcc, %12, v177\n\tv_cmp_eq_u32 %6, %12, v179\n\t"
                 "s_and_b64 %6, %6, vcc\n\ts_andn2_b64 %6, %6, %4\n\tv_cndmask_b32 %0, %0, v176, %6\n\tv_cndmask_b32 %1, %1, v178, %6\n\ts_or_b64 %4, %4, %6\n\t"
                 "v_cmp_eq_u32 vcc, %12, v181\n\tv_cmp_eq_u32 %6, %12, v183\n\ts_and_b64 %6, %6, vcc\n\ts_andn2_b64 %6, %6, %5\n\tv_cndmask_b32 %2, %2, v180, %6\n\t"
                 "v_cndmask_b32 %3, %3, v182, %6\n\ts_or_b64 %5, %5, %6\n\ts_and_b64 %6, %4, %5\n\ts_cmp_eq_u64 %6, -1\n\ts_cbranch_scc1 2f\n\t"
                 "v_cndmask_b32 %8, %9, 2.0, %4\n\tbuffer_load_dwordx4 v[176:179], %8, %10, %11 offen offset:0 sc1\n\tv_cndmask_b32 %8, %9, 2.0, %5\n\t"
                 "buffer_load_dwordx4 v[180:183], %8, %10, %11 offen offset:1024 sc1\n\ts_waitcnt vmcnt(4)\n\tv_cmp_eq_u32 vcc, %12, v185\n\tv_cmp_eq_u32 %6, %12, v187\n\t"
                 "s_and_b64 %6, %6, vcc\n\ts_andn2_b64 %6, %6, %4\n\tv_cndmask_b32 %0, %0, v184, %6\n\tv_cndmask_b32 %1, %1, v186, %6\n\ts_or_b64 %4, %4, %6\n\t"
                 "v_cmp_eq_u32 vcc, %12, v189\n\tv_cmp_eq_u32 %6, %12, v191\n\ts_and_b64 %6, %6, vcc\n\ts_andn2_b64 %6, %6, %5\n\tv_cndmask_b32 %2, %2, v188, %6\n\t"
                 "v_cndmask_b32 %3, %3, v190, %6\n\ts_or_b64 %5, %5, %6\n\ts_and_b64 %6, %4, %5\n\ts_cmp_eq_u64 %6, -1\n\ts_cbranch_scc1 2f\n\ts_sub_u32 %7, %7, 1\n\t"
                 "s_cbranch_scc0 1b\n\t2:\n.Lfused_tail2:"
                 : "+v"(v[0][0]), "+v"(v[0][1]), "+v"(v[1][0]), "+v"(v[1][1]), "+s"(ok[0]), "+s"(ok[1]), "=&s"(t), "+s"(spin), "=&v"(a)
                 : "v"(voff), "s"(r), "s"(soff), "s"(tag), "i"(GAP)
                 : "vcc", "scc", "memory", "v168", "v169", "v170", "v171", "v172", "v173", "v174", "v175", "v176", "v177", "v178", "v179", "v180", "v181", "v182", "v183", "v184", "v185", "v186", "v187", "v188", "v189", "v190", "v191");
  } else {
    asm volatile("v_cndmask_b32 %14, %15, 2.0, %8\n\tbuffer_load_dwordx4 v[168:171], %14, %16, %17 offen offset:0 sc1\n\tv_cndmask_b32 %14, %15, 2.0, %9\n\t"
                 "buffer_load_dwordx4 v[172:175], %14, %16, %17 offen offset:1024 sc1\n\tv_cndmask_b32 %14, %15, 2.0, %10\n\t"
                 "buffer_load_dwordx4 v[176:179], %14, %16, %17 offen offset:2048 sc1\n\tv_cndmask_b32 %14, %15, 2.0, %11\n\t"
                 "buffer_load_dwordx4 v[180:183], %14, %16, %17 offen offset:3072 sc1\n\ts_sleep %19\n\tv_cndmask_b32 %14, %15, 2.0, %8\n\t"
                 "buffer_load_dwordx4 v[184:187], %14, %16, %17 offen offset:0 sc1\n\tv_cndmask_b32 %14, %15, 2.0, %9\n\t"
                 "buffer_load_dwordx4 v[188:191], %14, %16, %17 offen offset:1024 sc1\n\tv_cndmask_b32 %14, %15, 2.0, %10\n\t"
                 "buffer_load_dwordx4 v[192:195], %14, %16, %17 offen offset:2048 sc1\n\tv_cndmask_b32 %14, %15, 2.0, %11\n\t"
                 "buffer_load_dwordx4 v[196:199], %14, %16, %17 offen offset:3072 sc1\n\ts_sleep %19\n\t1:\n\tv_cndmask_b32 %14, %15, 2.0, %8\n\t"
                 "buffer_load_dwordx4 v[200:203], %14, %16, %17 offen offset:0 sc1\n\tv_cndmask_b32 %14, %15, 2.0, %9\n\t"
                 "buffer_load_dwordx4 v[204:207], %14, %16, %17 offen offset:1024 sc1\n\tv_cndmask_b32 %14, %15, 2.0, %10\n\t"
                 "buffer_load_dwordx4 v[208:211], %14, %16, %17 offen offset:2048 sc1\n\tv_cndmask_b32 %14, %15, 2.0, %11\n\t"
                 "buffer_load_dwordx4 v[212:215], %14, %16, %17 offen offset:3072 sc1\n\ts_waitcnt vmcnt(8)\n\tv_cmp_eq_u32 vcc, %18, v169\n\t"
                 "v_cmp_eq_u32 %12, %18, v171\n\ts_and_b64 %12, %12, vcc\n\ts_andn2_b64 %12, %12, %8\n\tv_cndmask_b32 %0, %0, v168, %12\n\t"
                 "v_cndmask_b32 %1, %1, v170, %12\n\ts_or_b64 %8, %8, %12\n\tv_cmp_eq_u32 vcc, %18, v173\n\tv_cmp_eq_u32 %12, %18, v175\n\ts_and_b64 %12, %12, vcc\n\t"
                 "s_andn2_b64 %12, %12, %9\n\tv_cndmask_b32 %2, %2, v172, %12\n\tv_cndmask_b32 %3, %3, v174, %12\n\ts_or_b64 %9, %9, %12\n\tv_cmp_eq_u32 vcc, %18, v177\n\t"
                 "v_cmp_eq_u32 %12, %18, v179\n\ts_and_b64 %12, %12, vcc\n\ts_andn2_b64 %12, %12, %10\n\tv_cndmask_b32 %4, %4, v176, %12\n\t"
                 "v_cndmask_b32 %5, %5, v178, %12\n\ts_or_b64 %10, %10, %12\n\tv_cmp_eq_u32 vcc, %18, v181\n\tv_cmp_eq_u32 %12, %18, v183\n\ts_and_b64 %12, %12, vcc\n\t"
                 "s_andn2_b64 %12, %12, %11\n\tv_cndmask_b32 %6, %6, v180, %12\n\tv_cndmask_b32 %7, %7, v182, %12\n\ts_or_b64 %11, %11, %12\n\ts_and_b64 %12, %8, %9\n\t"
                 "s_and_b64 %12, %12, %10\n\ts_and_b64 %12, %12, %11\n\ts_cmp_eq_u64 %12, -1\n\ts_cbranch_scc1 2f\n\tv_cndmask_b32 %14, %15, 2.0, %8\n\t"
                 "buffer_load_dwordx4 v[168:171], %14, %16, %17 offen offset:0 sc1\n\tv_cndmask_b32 %14, %15, 2.0, %9\n\t"
                 "buffer_load_dwordx4 v[172:175], %14, %16, %17 offen offset:1024 sc1\n\tv_cndmask_b32 %14, %15, 2.0, %10\n\t"
                 "buffer_load_dwordx4 v[176:179], %14, %16, %17 offen offset:2048 sc1\n\tv_cndmask_b32 %14, %15, 2.0, %11\n\t"
                 "buffer_load_dwordx4 v[180:183], %14, %16, %17 offen offset:3072 sc1\n\ts_waitcnt vmcnt(8)\n\tv_cmp_eq_u32 vcc, %18, v185\n\t"
                 "v_cmp_eq_u32 %12, %18, v187\n\ts_and_b64 %12, %12, vcc\n\ts_andn2_b64 %12, %12, %8\n\tv_cndmask_b32 %0, %0, v184, %12\n\t"
                 "v_cndmask_b32 %1, %1, v186, %12\n\ts_or_b64 %8, %8, %12\n\tv_cmp_eq_u32 vcc, %18, v189\n\tv_cmp_eq_u32 %12, %18, v191\n\ts_and_b64 %12, %12, vcc\n\t"
                 "s_andn2_b64 %12, %12, %9\n\tv_cndmask_b32 %2, %2, v188, %12\n\tv_cndmask_b32 %3, %3, v190, %12\n\ts_or_b64 %9, %9, %12\n\tv_cmp_eq_u32 vcc, %18, v193\n\t"
                 "v_cmp_eq_u32 %12, %18, v195\n\ts_and_b64 %12, %12, vcc\n\ts_andn2_b64 %12, %12, %10\n\tv_cndmask_b32 %4, %4, v192, %12\n\t"
                 "v_cndmask_b32 %5, %5, v194, %12\n\ts_or_b64 %10, %10, %12\n\tv_cmp_eq_u32 vcc, %18, v197\n\tv_cmp_eq_u32 %12, %18, v199\n\ts_and_b64 %12, %12, vcc\n\t"
                 "s_andn2_b64 %12, %12, %11\n\tv_cndmask_b32 %6, %6, v196, %12\n\tv_cndmask_b32 %7, %7, v198, %12\n\ts_or_b64 %11, %11, %12\n\ts_and_b64 %12, %8, %9\n\t"
                 "s_and_b64 %12, %12, %10\n\ts_and_b64 %12, %12, %11\n\ts_cmp_eq_u64 %12, -1\n\ts_cbranch_scc1 2f\n\tv_cndmask_b32 %14, %15, 2.0, %8\n\t"
                 "buffer_load_dwordx4 v[184:187], %14, %16, %17 offen offset:0 sc1\n\tv_cndmask_b32 %14, %15, 2.0, %9\n\t"
                 "buffer_load_dwordx4 v[188:191], %14, %16, %17 offen offset:1024 sc1\n\tv_cndmask_b32 %14, %15, 2.0, %10\n\t"
                 "buffer_load_dwordx4 v[192:195], %14, %16, %17 offen offset:2048 sc1\n\tv_cndmask_b32 %14, %15, 2.0, %11\n\t"
                 "buffer_load_dwordx4 v[196:199], %14, %16, %17 offen offset:3072 sc1\n\ts_waitcnt vmcnt(8)\n\tv_cmp_eq_u32 vcc, %18, v201\n\t"
                 "v_cmp_eq_u32 %12, %18, v203\n\ts_and_b64 %12, %12, vcc\n\ts_andn2_b64 %12, %12, %8\n\tv_cndmask_b32 %0, %0, v200, %12\n\t"
                 "v_cndmask_b32 %1, %1, v202, %12\n\ts_or_b64 %8, %8, %12\n\tv_cmp_eq_u32 vcc, %18, v205\n\tv_cmp_eq_u32 %12, %18, v207\n\ts_and_b64 %12, %12, vcc\n\t"
                 "s_andn2_b64 %12, %12, %9\n\tv_cndmask_b32 %2, %2, v204, %12\n\tv_cndmask_b32 %3, %3, v206, %12\n\ts_or_b64 %9, %9, %12\n\tv_cmp_eq_u32 vcc, %18, v209\n\t"
                 "v_cmp_eq_u32 %12, %18, v211\n\ts_and_b64 %12, %12, vcc\n\ts_andn2_b64 %12, %12, %10\n\tv_cndmask_b32 %4, %4, v208, %12\n\t"
                 "v_cndmask_b32 %5, %5, v210, %12\n\ts_or_b64 %10, %10, %12\n\tv_cmp_eq_u32 vcc, %18, v213\n\tv_cmp_eq_u32 %12, %18, v215\n\ts_and_b64 %12, %12, vcc\n\t"
                 "s_andn2_b64 %12, %12, %11\n\tv_cndmask_b32 %6, %6, v212, %12\n\tv_cndmask_b32 %7, %7, v214, %12\n\ts_or_b64 %11, %11, %12\n\ts_and_b64 %12, %8, %9\n\t"
                 "s_and_b64 %12, %12, %10\n\ts_and_b64 %12, %12, %11\n\ts_cmp_eq_u64 %12, -1\n\ts_cbranch_scc1 2f\n\ts_sub_u32 %13, %13, 1\n\ts_cbranch_scc0 1b\n\t2:\n.Lfused_tail%20:"
                 : "+v"(v[0][0]), "+v"(v[0][1]), "+v"(v[1][0]), "+v"(v[1][1]), "+v"(v[2][0]), "+v"(v[2][1]), "+v"(v[3][0]), "+v"(v[3][1]), "+s"(ok[0]), "+s"(ok[1]), "+s"(ok[2]), "+s"(ok[3]), "=&s"(t), "+s"(spin), "=&v"(a)
                 : "v"(voff), "s"(r), "s"(soff), "s"(tag), "i"(GAP), "i"(ID)
                 : "vcc", "scc", "memory", "v168", "v169", "v170", "v171", "v172", "v173", "v174", "v175", "v176", "v177", "v178", "v179", "v180", "v181", "v182", "v183", "v184", "v185", "v186", "v187", "v188", "v189", "v190", "v191", "v192", "v193", "v194", "v195", "v196", "v197", "v198", "v199", "v200", "v201", "v202", "v203", "v204", "v205", "v206", "v207", "v208", "v209", "v210", "v211", "v212", "v213", "v214", "v215");
  }
}
template <int N, int SLEEP> __device__ __forceinline__ void gather(rsrc_t r, int voff, int soff, int stride, u32 tag, u32x4 (&g)[N]) {
  gather_issue<N>(r, voff, soff, stride, g);
  gather_wait<N, SLEEP>(r, voff, soff, stride, tag, g);
}
__device__ __forceinline__ u32 split_bf16(float x) {
  const float hi = (float)(__bf16)x;
  return pack_bf16(hi, x - hi);
}
__device__ __forceinline__ u32 dup_lo(u32 v) { return __builtin_amdgcn_perm(v, v, 0x01000100u); }
__device__ __forceinline__ u32 dup_hi(u32 v) { return __builtin_amdgcn_perm(v, v, 0x03020302u); }

template <int N> __device__ __forceinline__ f32x2 xw_partial(const u32x4 (&w)[N], const u32 (&x0)[N], const u32 (&x1)[N], int lane) {
  float a[16];
#pragma unroll
  for (int k = 0; k < 16; ++k) a[k] = 0.f;
#pragma unroll
  for (int j = 0; j < N; ++j) {
    const float xa = (lane & 8) ? hi2f(x0[j]) : lo2f(x0[j]), xb = (lane & 8) ? hi2f(x1[j]) : lo2f(x1[j]);
#pragma unroll
    for (int k = 0; k < 4; ++k) {
      const float lo = lo2f(w[j][k]), hi = hi2f(w[j][k]);
      a[2 * k] = fmaf(xa, lo, a[2 * k]); a[2 * k + 1] = fmaf(xa, hi, a[2 * k + 1]);
      a[8 + 2 * k] = fmaf(xb, lo, a[8 + 2 * k]); a[9 + 2 * k] = fmaf(xb, hi, a[9 + 2 * k]);
    }
  }
#pragma unroll
  for (int k = 0; k < 8; ++k) a[k] = merge32(a[k], a[k + 8]);
#pragma unroll
  for (int k = 0; k < 4; ++k) a[k] = merge16(a[k], a[k + 4]);
  return f32x2{merged<XOR8>(a[0], a[2], lane & 8), merged<XOR8>(a[1], a[3], lane & 8)};
}

__global__ void __launch_bounds__(512, 2)
fused_kernel(const unsigned short*, const unsigned short*, const unsigned short*, const unsigned short*, const unsigned short*,
             const unsigned short*, const unsigned short*, const unsigned short*, unsigned short*, unsigned char*) {
  __shared__ __attribute__((aligned(16))) float s_sc[8][68];
  __shared__ __attribute__((aligned(16))) float s_y[8][128];
  __shared__ __attribute__((aligned(16))) u32 s_o[8][320];
  __shared__ __attribute__((aligned(16))) u32 s_q[128];
  __shared__ __attribute__((aligned(16))) float s_qp[4][128];
  __shared__ __attribute__((aligned(16))) u32 s_h[128];
  __shared__ __attribute__((aligned(16))) float s_m[4];
  __shared__ __attribute__((aligned(16))) float s_l[4];
  __shared__ __attribute__((aligned(16))) float s_g[4];
  __shared__ __attribute__((aligned(16))) u32 s_cnt[4];

  u64x4 ka, kb;
  u64x2 kd;
  unsigned long long tnow;
  asm volatile("s_memrealtime %3\n\ts_load_dwordx8 %0, %4, 0x0\n\ts_load_dwordx8 %1, %4, 0x20\n\ts_load_dwordx4 %2, %4, 0x40"
               : "=&s"(ka), "=&s"(kb), "=&s"(kd), "=&s"(tnow) : "s"(__builtin_amdgcn_kernarg_segment_ptr()));
  const int tid = threadIdx.x, lane = tid & 63;
  u32 cv;
  {
    unsigned long long pc;
    u32 len;
    asm volatile("s_getpc_b64 %0\n1:\n\ts_max_i32 %1, .Lfused_tail-1b, 0\n\ts_max_i32 %1, .Lfused_tail2-1b, %1\n\ts_max_i32 %1, .Lfused_tail4-1b, %1\n\ts_max_i32 %1, .Lfused_tail5-1b, %1" : "=s"(pc), "=s"(len) :: "scc");
    asm volatile("buffer_load_dword %0, %1, %2, 0 offen" : "=v"(cv) : "v"((tid * CODESH + ((blockIdx.x >> 3) & (CODESH - 1))) * 128), "s"(mkrsrc((const void*)pc, len)));
  }
  SCHED_FENCE();

  const int wave = __builtin_amdgcn_readfirstlane(tid >> 6);
  const int xcd = blockIdx.x & 7, r = blockIdx.x >> 3;
  const int h = 2 * xcd + (r >> 4), mem = r & 15, b = mem >> 3, s = mem & 7;
  const int bh = b * H + h;
  const int rh = mem >= 7 ? 1 : 0, cb = mem - 7 * rh;

  const int g8 = lane & 7, c8 = lane >> 3;
  const int q4 = lane >> 4, l16 = lane & 15;
  u32x2 wov[4], w2v[2];
  u32 tag;
  u32 fbase, fsize, fx1, fx2, fx4, fc, fsh;
  asm volatile("s_cmp_lt_u32 %7, 4\n\ts_cbranch_scc0 1f\n\ts_setprio 3\n1:\n\t"
               "s_cselect_b32 %0, %8, %9\n\ts_cselect_b32 %5, %10, %11\n\ts_cselect_b32 %6, 13, 7\n\t"
               "s_mov_b32 %1, 0x200000\n\ts_cmov_b32 %1, 0xe0000\n\ts_mov_b32 %2, %12\n\ts_cmov_b32 %2, 0x10000\n\t"
               "s_mov_b32 %3, %13\n\ts_cmov_b32 %3, 0x20000\n\ts_cselect_b32 %4, 2.0, %14"
               : "=&s"(fbase), "=&s"(fsize), "=&s"(fx1), "=&s"(fx2), "=&s"(fx4), "=&s"(fc), "=&s"(fsh)
               : "s"(wave), "s"((mem >> 2) * 112 * H * DH * 2 + h * 512 + (mem & 3) * 128), "s"(bh * SK * DH * 2), "s"(wave << 18),
                 "s"((wave - 4) * 1024 + s * 4096), "i"(block_of(1)), "i"(block_of(2)), "i"(block_of(4)) : "scc");
  int fo[6];
  fo[0] = (c8 << fsh) + g8 * 16 + fc;
  fo[1] = fo[0] ^ fx1; fo[2] = fo[0] ^ fx2; fo[3] = fo[1] ^ fx2; fo[4] = fo[0] ^ fx4; fo[5] = fo[1] ^ fx4;
  asm volatile("" : "+v"(fo[0]), "+v"(fo[1]), "+v"(fo[2]), "+v"(fo[3]), "+v"(fo[4]), "+v"(fo[5]));
  SCHED_FENCE();
  asm volatile("s_waitcnt lgkmcnt(0)" : "+s"(ka), "+s"(kb), "+s"(kd), "+s"(tnow));
  typedef const unsigned short* cptr;
  const cptr x = (cptr)ka[0], kc = (cptr)ka[1], vc = (cptr)ka[2], wq = (cptr)ka[3], wo = (cptr)kb[0], w1 = (cptr)kb[1], w2 = (cptr)kb[2], gw = (cptr)kb[3];
  const rsrc_t rout = mkrsrc((const void*)kd[0], 2 * D * 2), rws = mkrsrc((const void*)kd[1], WS_END);
  const u32 tstart = (u32)tnow;
  auto load_proj = [&](int k) {
    const rsrc_t rwo = mkrsrc(wo + (size_t)(h * DH + rh * 128 + wave * 16) * D, mem < 14 ? 16 * D * 2 : 0);
    const rsrc_t rw2 = mkrsrc(w2 + (size_t)(h < 14 ? h * 128 + rh * 64 + wave * 8 : 0) * D, mem < 14 && h < 14 ? 8 * D * 2 : 0);
    if (k < 2) w2v[k] = ld8<AUX_NT>(rw2, 2 * q4 * (D * 2) + cb * 128 + l16 * 8, k * D * 2);
    else wov[k - 2] = ld8<AUX_NT>(rwo, 4 * q4 * (D * 2) + cb * 128 + l16 * 8, (k - 2) * D * 2);
  };

  const size_t base = (size_t)bh * SK * DH;
  u32x4 first[PRE];
  {
    const rsrc_t rf = mkrsrc((const char*)(wave < 4 ? wq : kc) + fbase, fsize);
#pragma unroll
    for (int j = 0; j < PRE; ++j) first[j] = ld16<AUX_NT>(rf, fo[j], 0);
  }
  SCHED_FENCE();
  const rsrc_t rx = mkrsrc(x, 2 * D * 2);
  u32 x0[4], x1[4], ev;
  if (wave >= 4) {
    const int sw = wave - 4;
    const rsrc_t rk = mkrsrc(kc + base, SK * DH * 2), rv = mkrsrc(vc + base, SK * DH * 2);
    const int koff = (tid - 256) * 16;
    const int voff = c8 * 512 + sw * 128 + g8 * 16;
    u32x4 buf[WIN];
    u32 t0 = tstart;
    int sr = s * 4096;
    auto issue = [&](int n) {
      if (n < 128) {
        if (n % 2 == 0) {
          asm volatile("1:\n\ts_memrealtime vcc\n\ts_waitcnt lgkmcnt(0)\n\ts_sub_u32 vcc_lo, vcc_lo, %0\n\ts_cmpk_lt_u32 vcc_lo, %1\n\ts_cbranch_scc1 1b\n\t"
                       ".set .Llv, %2\n\t.rept %2\n\ts_cmpk_ge_u32 vcc_lo, %1 + .Llv * %3\n\ts_cbranch_scc1 2f\n\ts_waitcnt vmcnt(%4 + .Llv)\n\t"
                       ".set .Llv, .Llv - 1\n\t.endr\n2:"
                       :: "s"(t0), "i"(sched(n)), "i"(WLEVELS), "i"(WSTEP), "i"(W0 - 2) : "vcc", "scc", "memory");
        }
        if (n >= WMAX0) asm volatile("s_waitcnt vmcnt(%0)" :: "i"((n < 32 ? WMAX0 : WMAX) - 1) : "memory");
        const int so = sr ^ block_of(n & 63);
        asm volatile("" : "+s"(sr));
        buf[n & 31] = n < 64 ? ld16<AUX_NT>(rk, koff, so) : ld16<AUX_NT>(rv, voff, so);
      }
    };
#pragma unroll
    for (int i = 0; i < PRE; ++i) buf[i] = first[i];
    asm volatile("s_barrier" ::: "memory");
#pragma unroll
    for (int i = PRE; i < WIN; ++i) { issue(i); SCHED_FENCE(); }
    asm volatile("" :: "v"(cv));
    wg_barrier();
    const u32x4 qv = *(const u32x4*)&s_q[(lane & 31) * 4];
    const u32x2 tt = *(const u32x2*)&s_cnt[2];
    t0 = __builtin_amdgcn_readfirstlane(tt[0]);
    tag = __builtin_amdgcn_readfirstlane(tt[1]);
    float sc[2];
#pragma unroll
    for (int blk = 0; blk < 2; ++blk) {
      float lv[5];
#pragma unroll
      for (int i = 0; i < 32; ++i) {
        const u32x4 kv = buf[i];
        float d = dot2(kv[0], qv[0], 0.f); d = dot2(kv[1], qv[1], d); d = dot2(kv[2], qv[2], d); d = dot2(kv[3], qv[3], d);
        asm volatile("" : "+v"(d));
        SCHED_FENCE();
        if (blk == 0) { if ((i + 1) * LAG / 32 != i * LAG / 32) issue(32 + i * LAG / 32); }
        else issue(32 + i + LAG);
        SCHED_FENCE();
        if (i & 1) {
          d = merge16(lv[0], d);
          if (i & 2) {
            d = merged<XOR8>(lv[1], d, lane & 8);
            if (i & 4) {
              d = merged<XOR7>(lv[2], d, lane & 4);
              if (i & 8) {
                d = merged<XOR2>(lv[3], d, lane & 2);
                if (i & 16) sc[blk] = merged<XOR1>(lv[4], d, lane & 1);
                else lv[4] = d;
              } else lv[3] = d;
            } else lv[2] = d;
          } else lv[1] = d;
        } else lv[0] = d;
      }
    }
    constexpr float C2 = 0.0625f * 1.4426950408889634f;
    sc[0] *= C2; sc[1] *= C2;
    const int ir = __builtin_bitreverse32((u32)(lane & 31)) >> 27;
    s_sc[2 * sw + (lane >> 5)][ir] = sc[0];
    s_sc[2 * sw + (lane >> 5)][32 + ir] = sc[1];
    const float m = wave_max(fmaxf(sc[0], sc[1]));
    const float l = wave_sum(__builtin_amdgcn_exp2f(sc[0] - m) + __builtin_amdgcn_exp2f(sc[1] - m));
    if (lane == 0) { s_m[sw] = m; s_l[sw] = l; }
    wg_barrier();
    const f32x4 mv = *(const f32x4*)s_m;
    const float M = fmaxf(fmaxf(mv[0], mv[1]), fmaxf(mv[2], mv[3]));
    float acc[8], F;
    int ooff = (lane & 8) ? OOB : (g8 >> 1) * 2048 + (4 * (g8 & 1) + 2 * (lane >> 5) + ((lane >> 4) & 1)) * 16;
#pragma unroll
    for (int j = 0; j < 8; ++j) acc[j] = 0.f;
#pragma unroll
    for (int i4 = 0; i4 < 16; ++i4) {
      const f32x4 sv = *(const f32x4*)&s_sc[c8][i4 * 4];
#pragma unroll
      for (int ii = 0; ii < 4; ++ii) {
        const int i = i4 * 4 + ii;
        const float p = __builtin_amdgcn_exp2f(sv[ii] - M);
        const u32x4 vv = buf[i & 31];
#pragma unroll
        for (int j = 0; j < 4; ++j) { acc[2 * j] = fmaf(p, lo2f(vv[j]), acc[2 * j]); acc[2 * j + 1] = fmaf(p, hi2f(vv[j]), acc[2 * j + 1]); }
#pragma unroll
        for (int j = 0; j < 8; ++j) asm volatile("" : "+v"(acc[j]));
        SCHED_FENCE();
        if (i == 0 || vload(i) != vload(i - 1)) issue(vload(i));
        if (i == VLAST) {
          asm volatile("s_barrier" ::: "memory");
          F = s_g[2];
          asm volatile("" : "+v"(F), "+v"(ooff));
        }
        if (i >= VLAST + 2 && i < VLAST + 8) load_proj(i - VLAST - 2);
        SCHED_FENCE();
      }
    }
    float t[4], u[2];
#pragma unroll
    for (int k = 0; k < 4; ++k) t[k] = merge32(acc[k], acc[k + 4]);
#pragma unroll
    for (int k = 0; k < 2; ++k) { u[k] = merge16(t[k], t[k + 2]); u[k] = (u[k] + dpp<XOR8>(u[k])) * F; }
    st16_sc1(rws, ooff, WS_O + h * 32768 + sw * 8192 + mem * 128, u32x4{split_bf16(u[0]), tag, split_bf16(u[1]), tag});

  } else {
#pragma unroll
    for (int j = 0; j < 4; ++j) {
      const int xo = ((mem >> 2) * 112 + min(wave * 32 + j * 8 + (c8 & 6), 110)) * 2;
      asm volatile("buffer_load_dword %0, %2, %3, 0 offen\n\tbuffer_load_dword %1, %2, %3, 0 offen offset:896" : "=&v"(x0[j]), "=&v"(x1[j]) : "v"(xo), "s"(rx));
    }
    asm volatile("buffer_load_dword %0, off, %1, 0" : "=v"(ev) : "s"(rws));
    if (tid == 0) s_cnt[0] = 0;
    SCHED_FENCE();
    wg_barrier();
    int go = 1;
    asm volatile("" : "+s"(go));
    if (__builtin_expect(go != 0, 0)) {
    const int hw = wave;
    {
      u32x4 wv[4];
#pragma unroll
      for (int j = 0; j < 4; ++j) wv[j] = first[j];
      asm volatile("s_waitcnt vmcnt(0)" : "+v"(cv), "+v"(x0[0]), "+v"(x1[0]), "+v"(x0[1]), "+v"(x1[1]), "+v"(x0[2]), "+v"(x1[2]), "+v"(x0[3]), "+v"(x1[3]), "+v"(ev));
      *(f32x2*)&s_qp[hw][lane * 2] = xw_partial<4>(wv, x0, x1, lane);
      tag = __builtin_amdgcn_readfirstlane(ev) + 1u;
    }
    asm volatile("s_waitcnt lgkmcnt(0)" ::: "memory");
    const int poff = (lane >> 5) * 512 + g8 * 64 + ((lane >> 3) & 3) * 16;
    if (hw != 0) {
      if (lane == 0) __hip_atomic_fetch_add(&s_cnt[0], 1u, __ATOMIC_RELAXED, __HIP_MEMORY_SCOPE_WORKGROUP);
    } else {
#pragma nounroll
      while (__hip_atomic_load(&s_cnt[0], __ATOMIC_RELAXED, __HIP_MEMORY_SCOPE_WORKGROUP) != 3u) {}
      asm volatile("" ::: "memory");
      f32x2 q2 = *(const f32x2*)&s_qp[0][lane * 2];
#pragma unroll
      for (int w = 1; w < 4; ++w) q2 += *(const f32x2*)&s_qp[w][lane * 2];
      st16_sc1(rws, (lane >> 5) * 8192 + (poff & 511), WS_Q + h * 16384 + (mem & 2) * 2048 + (mem >> 2) * 1024 + (mem & 1) * 512,
               u32x4{__float_as_uint(q2[0]), tag, __float_as_uint(q2[1]), tag});
    }
    if (hw == 3) {
      st16_sc1(rws, r == 0 && lane < 8 ? xcd * 128 + lane * 16 : OOB, WS_T0, u32x4{tstart, tag, tstart, tag});
      asm volatile("s_sleep 46" ::: "memory");
      u32x4 gt[1];
      gather<1, 4>(rws, lane * 16, WS_T0, 0, tag, gt);
      const float dref = wave_sum((float)(int)(gt[0][0] - tstart)) * (1.f / 64.f);
      if (lane == 0) *(u32x2*)&s_cnt[2] = u32x2{tstart + (u32)(int)dref, tag};
    }
    if (hw == 1 || hw == 2) {
      asm volatile("s_sleep 46" ::: "memory");
      u32x4 gq[4];
      u32 vq[4][2];
      const int qbase = WS_Q + h * 16384 + b * 8192 + (hw - 1) * 4096;
      gather_issue<4>(rws, lane * 16, qbase, 1024, gq);
      gather_wait3<4, 10, 5>(rws, lane * 16, qbase, tag, gq, vq);
      s_q[(hw - 1) * 64 + lane] = pack_bf16((__uint_as_float(vq[0][0]) + __uint_as_float(vq[1][0])) + (__uint_as_float(vq[2][0]) + __uint_as_float(vq[3][0])),
                                            (__uint_as_float(vq[0][1]) + __uint_as_float(vq[1][1])) + (__uint_as_float(vq[2][1]) + __uint_as_float(vq[3][1])));
    }
    wg_barrier();
    if (hw == 3) {
      const rsrc_t rw1 = mkrsrc(w1 + (size_t)(mem >> 1) * 56 * DFF, h < 14 ? 56 * DFF * 2 : 0);
      u32 x0[7], x1[7];
      u32x4 wv[7];
#pragma unroll
      for (int j = 0; j < 7; ++j) {
        wv[j] = ld16<AUX_NT>(rw1, (j * 8 + c8) * (DFF * 2) + h * 256 + (mem & 1) * 128 + g8 * 16, 0);
        const int xo = ((mem >> 1) * 56 + j * 8 + (c8 & 6)) * 2;
        x0[j] = __builtin_amdgcn_raw_buffer_load_b32(rx, xo, 0, 0);
        x1[j] = __builtin_amdgcn_raw_buffer_load_b32(rx, xo, D * 2, 0);
      }
      const f32x2 hp = xw_partial<7>(wv, x0, x1, lane);
      st16_sc1(rws, poff, WS_H + h * 16384 + (mem & 1) * 8192 + (mem >> 1) * 1024, u32x4{__float_as_uint(hp[0]), tag, __float_as_uint(hp[1]), tag});
    } else if (hw == 1 || hw == 2) {
      const rsrc_t rg = mkrsrc(gw, D * 2);
      const u32x4 xa = ld16<0>(rx, lane < 56 ? lane * 16 : OOB, (hw - 1) * D * 2), ga = ld16<0>(rg, lane * 16, 0);
      float z = dot2(xa[0], ga[0], 0.f); z = dot2(xa[1], ga[1], z); z = dot2(xa[2], ga[2], z); z = dot2(xa[3], ga[3], z);
      z = wave_sum(z);
      if (lane == 0) s_g[hw - 1] = 1.f / (1.f + __expf(-z));
    } else {
      u32 tv = __builtin_amdgcn_raw_buffer_load_b32(mkrsrc(wo + (size_t)(h * DH + rh * 128) * D, 128 * D * 2), lane * 16384, 0, AUX_NT);
      tv ^= __builtin_amdgcn_raw_buffer_load_b32(mkrsrc(w2 + (size_t)(h < 14 ? h * 128 + rh * 64 : 0) * D, 64 * D * 2), lane * 16384, 0, AUX_NT);
      tv ^= __builtin_amdgcn_raw_buffer_load_b32(mkrsrc(vc + base, SK * DH * 2), lane ? OOB : s * 4096, 0, AUX_NT);
      asm volatile("" :: "v"(tv));
    }
    const u32 t0 = __builtin_amdgcn_readfirstlane(s_cnt[2]);
    wg_barrier();
    if (hw == 0) {
      const f32x4 mv = *(const f32x4*)s_m, lv = *(const f32x4*)s_l;
      const float M = fmaxf(fmaxf(mv[0], mv[1]), fmaxf(mv[2], mv[3]));
      float L = 0.f;
#pragma unroll
      for (int w = 0; w < 4; ++w) L = fmaf(__builtin_amdgcn_exp2f(mv[w] - M), lv[w], L);
      st16_sc1(rws, lane < 8 ? lane * 16 : OOB, WS_ML + bh * 1024 + s * 128, u32x4{__float_as_uint(M), tag, __float_as_uint(L), tag});
      asm volatile("s_sleep 16" ::: "memory");
      wait_until<T_ML>(t0);
      u32x4 gm[1];
      gather<1, 30>(rws, lane * 16, WS_ML + bh * 1024, 0, tag, gm);
      const float ms = __uint_as_float(gm[0][0]);
      const float MM = max32(max16(fmaxf(ms, dpp<XOR8>(ms))));
      float LL = __builtin_amdgcn_exp2f(ms - MM) * __uint_as_float(gm[0][2]);
      LL += dpp<XOR8>(LL); LL = merge16(LL, LL); LL = merge32(LL, LL);
      if (lane == 0) s_g[2] = s_g[b] * __builtin_amdgcn_exp2f(M - MM) / LL;
    } else if (hw == 3) {
      u32x4 gh[8];
      gather<8, 8>(rws, lane * 16, WS_H + h * 16384 + rh * 8192, 1024, tag, gh);
      f32x2 hs = {0.f, 0.f};
#pragma unroll
      for (int k = 0; k < 8; ++k) hs += f32x2{__uint_as_float(gh[k][0]), __uint_as_float(gh[k][2])};
      const float ng = 1.f - s_g[lane >> 5];
      *(u32x2*)&s_h[lane * 2] = u32x2{split_bf16(fmaxf(hs[0], 0.f) * ng), split_bf16(fmaxf(hs[1], 0.f) * ng)};
    } else if (hw == 1) {
      asm volatile("s_sleep 100" ::: "memory");
      const int to = lane < 16 ? WS_O + h * 32768 + lane * 2048 + mem * 128
                   : lane < 24 && mem < 14 ? WS_Y + ((lane >> 2 & 1) * 28 + cb * 4 + (lane & 3)) * 4096 + (h * 2 + rh) * 128 : OOB;
      u32 tv = __builtin_amdgcn_raw_buffer_load_b32(rws, to, 0, AUX_SC1);
      asm volatile("" :: "v"(tv));
    } else if (blockIdx.x == 0) {
      wait_until<T_ML>(t0);
      u32x4 ge[1];
      gather<1, 30>(rws, l16 * 1024, WS_ML, 0, tag, ge);
      __builtin_amdgcn_raw_buffer_store_b32(tag, rws, lane == 0 ? WS_EPOCH : OOB, 0, AUX_SC1);
    }
    if (hw < 2) {
      wait_until<T_PROJ>(t0 + hw * 80);
#pragma unroll
      for (int k = 0; k < 6; ++k) load_proj(k);
    }
    wg_barrier();
    if (hw >= 2) {
      asm volatile("s_sleep 2" ::: "memory");
#pragma unroll
      for (int k = 0; k < 6; ++k) { asm volatile("s_sleep 3" ::: "memory"); load_proj(k); }
    }
    asm volatile(".Lfused_tail:" ::: "memory");
    }
  }
  SCHED_FENCE();
  asm volatile("s_barrier" ::: "memory");
  const u32 t1 = __builtin_amdgcn_readfirstlane(s_cnt[2]), utag = __builtin_amdgcn_readfirstlane(tag);

  if (mem < 14) {
    asm volatile("s_sleep 2" ::: "memory");
    wait_until<T_O>(t1);
    u32x4 go[2];
    const int goff = lane * 16, gbase = WS_O + h * 32768 + (rh * 8 + wave) * 2048;
    gather_issue<2>(rws, goff, gbase, 1024, go);
    SCHED_FENCE();
    u32x2 ha = *(const u32x2*)&s_h[(l16 >> 3) * 64 + wave * 8 + 2 * q4];
    if (lane & 7) ha = u32x2{0u, 0u};
    f32x4 ya[4];
    u32x4 wb[4];
#pragma unroll
    for (int c = 0; c < 4; ++c) {
      const u32x2 hb = (c & 1) ? u32x2{dup_hi(w2v[0][c >> 1]), dup_hi(w2v[1][c >> 1])} : u32x2{dup_lo(w2v[0][c >> 1]), dup_lo(w2v[1][c >> 1])};
      ya[c] = __builtin_amdgcn_mfma_f32_16x16x16bf16_1k(__builtin_bit_cast(s16x4, ha), __builtin_bit_cast(s16x4, hb), f32x4{0.f, 0.f, 0.f, 0.f}, 0, 0, 0);
#pragma unroll
      for (int i = 0; i < 4; ++i) wb[c][i] = (c & 1) ? dup_hi(wov[i][c >> 1]) : dup_lo(wov[i][c >> 1]);
    }
    int yw = (wave * 128 + (lane >> 5) * 64 + l16 * 4 + (q4 & 1) * 2) * 4;
    int yoff = ((lane >> 5) * 28 + ((lane >> 3) & 3)) * 4096 + g8 * 16, ybase = WS_Y + cb * 16384 + (h * 2 + rh) * 128;
#pragma unroll
    for (int c = 0; c < 4; ++c) asm volatile("" : "+v"(ya[c]), "+v"(wb[c]));
    int ow = wave * 1280 + c8 * 80 + g8 * 8, orr = wave * 1280 + l16 * 80 + q4 * 16;
    asm volatile("" : "+v"(yw), "+v"(yoff), "+s"(ybase), "+v"(ow), "+v"(orr));
    SCHED_FENCE();
    u32 vo[2][2];
    gather_wait3<2, 4, 2>(rws, goff, gbase, utag, go, vo);
    *(u32x2*)((char*)s_o + ow) = u32x2{vo[0][0], vo[0][1]};
    *(u32x2*)((char*)s_o + ow + 640) = u32x2{vo[1][0], vo[1][1]};
    asm volatile("" ::: "memory");
    const u32x4 oa = *(const u32x4*)((const char*)s_o + orr);
    float t[4];
#pragma unroll
    for (int c = 0; c < 4; ++c)
      ya[c] = __builtin_amdgcn_mfma_f32_16x16x32_bf16(__builtin_bit_cast(bf16x8, oa), __builtin_bit_cast(bf16x8, wb[c]), ya[c], 0, 0, 0);
    SCHED_FENCE();
#pragma unroll
    for (int c = 0; c < 4; ++c) {
      const f32x2 p = f32x2{ya[c][0], ya[c][1]} + f32x2{ya[c][2], ya[c][3]};
      t[c] = p[0] + p[1];
      asm volatile("" : "+v"(t[c]));
    }
    float y0 = merge16(t[0], t[2]), y1 = merge16(t[1], t[3]);
    asm volatile("" : "+v"(y0));
    *(f32x2*)((char*)s_y + yw) = f32x2{y0, y1};
    wg_barrier();
    if (wave == 0) {
      f32x2 p[8];
#pragma unroll
      for (int w = 0; w < 8; ++w) p[w] = *(const f32x2*)&s_y[w][lane * 2];
      const f32x2 y = ((p[0] + p[1]) + (p[2] + p[3])) + ((p[4] + p[5]) + (p[6] + p[7]));
      st16_sc1(rws, yoff, ybase, u32x4{__float_as_uint(y[0]), tag, __float_as_uint(y[1]), tag});
    }
  } else if (wave < 2 && h < 14) {
    const int cq = h * 4 + (mem & 1) * 2 + wave;
    asm volatile("s_sleep 34" ::: "memory");
    wait_until<T_Y>(t1);
    u32x4 gy[4];
    u32 vy[4][2];
    gather_issue<4>(rws, lane * 16, WS_Y + cq * 4096, 1024, gy);
    gather_wait3<4, 4, 4>(rws, lane * 16, WS_Y + cq * 4096, utag, gy, vy);
    float y0 = (__uint_as_float(vy[0][0]) + __uint_as_float(vy[1][0])) + (__uint_as_float(vy[2][0]) + __uint_as_float(vy[3][0]));
    float y1 = (__uint_as_float(vy[0][1]) + __uint_as_float(vy[1][1])) + (__uint_as_float(vy[2][1]) + __uint_as_float(vy[3][1]));
    y0 += dpp<XOR8>(y0); y0 = merge16(y0, y0); y0 = merge32(y0, y0);
    y1 += dpp<XOR8>(y1); y1 = merge16(y1, y1); y1 = merge32(y1, y1);
    __builtin_amdgcn_raw_buffer_store_b32(pack_bf16(y0, y1), rout, lane < 8 ? cq * 32 + lane * 4 : OOB, 0, 0);
  }
}

extern "C" void kernel_launch(void* const* d_in, const int* in_sizes, int n_in,
                              void* d_out, int out_size, void* d_ws, size_t ws_size,
                              hipStream_t stream) {
  fused_kernel<<<256, 512, 0, stream>>>((const unsigned short*)d_in[0], (const unsigned short*)d_in[1], (const unsigned short*)d_in[2],
                                        (const unsigned short*)d_in[3], (const unsigned short*)d_in[4], (const unsigned short*)d_in[6],
                                        (const unsigned short*)d_in[7], (const unsigned short*)d_in[8], (unsigned short*)d_out,
                                        (unsigned char*)d_ws);
}
```
